```python
import torch

def _softmax(x, axis=-1):
    return torch.softmax(x, dim=axis)
def _gelu(x):
    return torch.nn.functional.gelu(x, approximate="tanh")
def _silu(x):
    return torch.nn.functional.silu(x)
def _sigmoid(x):
    return torch.sigmoid(x)
def _layernorm(x, axis=-1, eps=1e-5):
    m = x.mean(dim=axis, keepdim=True)
    v = x.var(dim=axis, keepdim=True, unbiased=False)
    return (x - m) / torch.sqrt(v + eps)
def _scatter_add(tgt, idx, src, axis=0):
    return tgt.clone().index_add_(axis, idx, src)
def _segment_sum(src, seg, n):
    out = torch.zeros((n,) + src.shape[1:], dtype=src.dtype)
    return out.index_add_(0, seg, src)
def _pad(x, pw):
    flat = tuple(v for pair in reversed(pw) for v in pair)
    return torch.nn.functional.pad(x, flat)

kernel_name = 'recurrent_scan__mha_block_cached__glu_gated_swiglu__decode'

def setup_inputs():

    _w = (lambda a: (lambda t: t.to(torch.bfloat16) if t.is_floating_point() else t)(torch.as_tensor(a)))
    def _A_setup():
        B, Sq, D = 8, 1, 576
        H, Sk, Dh = 32, 4096, 128
        return {
            "x": _w((lambda *s: torch.randn(*s, dtype=torch.bfloat16) * 0.05)(B, Sq, D)),
            "k_cache": _w((lambda *s: torch.randn(*s, dtype=torch.bfloat16) * 3.2)(B, H, Sk, Dh)),
            "v_cache": _w((lambda *s: torch.randn(*s, dtype=torch.bfloat16) * 3.2)(B, H, Sk, Dh)),
            "wq": _w((lambda *s: torch.randn(*s, dtype=torch.bfloat16) * 3.2)(D, H * Dh) * (D ** -0.5)),
            "wo": _w((lambda *s: torch.randn(*s, dtype=torch.bfloat16) * 3.2)(H * Dh, D) * ((H * Dh) ** -0.5)),
        }
    _A_in = _A_setup()

    def _B_setup():
        B, S, D, F = 8, 1, 576, 4608
        return {
            "x": _w((lambda *s: torch.randn(*s, dtype=torch.bfloat16) * 0.05)(B, S, D)),
            "w1": _w((lambda *s: torch.randn(*s, dtype=torch.bfloat16) * 0.4)(D, F) * (2.0 / D) ** 0.5),
            "w3": _w((lambda *s: torch.randn(*s, dtype=torch.bfloat16) * 0.4)(D, F) * (2.0 / D) ** 0.5),
            "w2": _w((lambda *s: torch.randn(*s, dtype=torch.bfloat16) * 0.4)(F, D) * (2.0 / F) ** 0.5),
        }
    _B_in = _B_setup()

    return {
        "A_x": _A_in["x"],
        "A_k_cache": _A_in["k_cache"],
        "A_v_cache": _A_in["v_cache"],
        "A_wq": _A_in["wq"],
        "A_wo": _A_in["wo"],
        "B_x": _B_in["x"],
        "B_w1": _B_in["w1"],
        "B_w3": _B_in["w3"],
        "B_w2": _B_in["w2"],
    }

def reference(A_x, A_k_cache, A_v_cache, A_wq, A_wo, B_x, B_w1, B_w3, B_w2):
    def _A_ref(x, k_cache, v_cache, wq, wo):
        B, Sq, D = x.shape
        H, Dh = 32, 128
        q = (lambda x, *d: x.permute(*d))((x @ wq).reshape(B, Sq, H, Dh), 0, 2, 1, 3)
        scores = torch.einsum("bhqd,bhkd->bhqk", q, k_cache) * (Dh ** -0.5)
        p = _softmax(scores, axis=-1)
        o = torch.einsum("bhqk,bhkd->bhqd", p, v_cache)
        return (lambda x, *d: x.permute(*d))(o, 0, 2, 1, 3).reshape(B, Sq, H * Dh) @ wo

    def _B_ref(x, w1, w3, w2):
        gate = torch.einsum("bsd,df->bsf", x, w1)
        up = torch.einsum("bsd,df->bsf", x, w3)
        h = _silu(gate) * up
        out = torch.einsum("bsf,fd->bsd", h, w2)
        return out

    x = A_x
    xn = _layernorm(x, axis=-1)
    s = _A_ref(xn, A_k_cache, A_v_cache, A_wq, A_wo)
    g = _silu(_B_ref(xn, B_w1, B_w3, B_w2))
    return x + s * g
```

```cpp
#include <hip/hip_runtime.h>
#include <hip/hip_bf16.h>
#include <stdint.h>

#define MIMIC 1

typedef unsigned short u16;
typedef unsigned u32x4 __attribute__((ext_vector_type(4)));
typedef float f32x4 __attribute__((ext_vector_type(4)));
typedef __bf16 bf16x8 __attribute__((ext_vector_type(8)));

constexpr int B = 8, D = 576, H = 32, SK = 4096, DH = 128, F = 4608, HD = H * DH;

__device__ __forceinline__ float bf2f(u16 x) { return __uint_as_float(((unsigned)x) << 16); }
__device__ __forceinline__ u16 f2bf(float x) { return __builtin_bit_cast(u16, (__bf16)x); }
__device__ __forceinline__ float rbf(float x) { return bf2f(f2bf(x)); }
__device__ __forceinline__ float mrbf(float x) { return MIMIC ? rbf(x) : x; }

__device__ __forceinline__ float wave_sum(float v) {
  for (int o = 32; o > 0; o >>= 1) v += __shfl_xor(v, o);
  return v;
}
__device__ __forceinline__ float wave_max(float v) {
  for (int o = 32; o > 0; o >>= 1) v = fmaxf(v, __shfl_xor(v, o));
  return v;
}

__device__ __forceinline__ void ln_row(const u16* x, int b, int lane, float* xn) {
  float v[9]; float s = 0.f;
  for (int i = 0; i < 9; ++i) { v[i] = bf2f(x[b * D + lane + 64 * i]); s += v[i]; }
  s = wave_sum(s);
  const float mean = s * (1.0f / D);
  float q = 0.f;
  for (int i = 0; i < 9; ++i) { float d = v[i] - mean; q += d * d; }
  q = wave_sum(q);
  const float var = rbf(q * (1.0f / D));
  const float m = rbf(mean);
  const float den = rbf(sqrtf(rbf(var + 1e-5f)));
  for (int i = 0; i < 9; ++i) xn[b * D + lane + 64 * i] = rbf(rbf(v[i] - m) / den);
}

__device__ __forceinline__ float silu(float x) { return x / (1.0f + __expf(-x)); }

__global__ void __launch_bounds__(512) k_pre(const u16* x, const u16* wq, const u16* w1, const u16* w3, u16* q, u16* h) {
  __shared__ float xn[B * D];
  __shared__ float red[2][8][B][64];
  const int tid = threadIdx.x, lane = tid & 63, w = tid >> 6;
  ln_row(x, w, lane, xn);
  __syncthreads();
  const bool isq = blockIdx.x < 64;
  const int col = (isq ? blockIdx.x : blockIdx.x - 64) * 64 + lane;
  const u16* wa = isq ? wq : w1; const int ld = isq ? HD : F;
  float a[B], c[B];
  for (int b = 0; b < B; ++b) a[b] = c[b] = 0.f;
  for (int k = w * 72; k < w * 72 + 72; ++k) {
    const float wv = bf2f(wa[(size_t)k * ld + col]);
    const float uv = isq ? 0.f : bf2f(w3[(size_t)k * ld + col]);
    for (int b = 0; b < B; ++b) { a[b] += xn[b * D + k] * wv; c[b] += xn[b * D + k] * uv; }
  }
  for (int b = 0; b < B; ++b) { red[0][w][b][lane] = a[b]; red[1][w][b][lane] = c[b]; }
  __syncthreads();
  float sa = 0.f, sc = 0.f;
  for (int i = 0; i < 8; ++i) { sa += red[0][i][w][lane]; sc += red[1][i][w][lane]; }
  if (isq) q[w * HD + col] = f2bf(sa);
  else h[w * F + col] = f2bf(rbf(silu(rbf(sa))) * rbf(sc));
}

__device__ __forceinline__ f32x4 mfma16(u32x4 a, u32x4 b, f32x4 c) {
  return __builtin_amdgcn_mfma_f32_16x16x32_bf16(__builtin_bit_cast(bf16x8, a), __builtin_bit_cast(bf16x8, b), c, 0, 0, 0);
}
template <int N> __device__ __forceinline__ unsigned bcast16(unsigned x) {
  return (unsigned)__builtin_amdgcn_update_dpp(0, (int)x, 0x150 + N, 0xf, 0xf, true);
}

constexpr int NW = 8, KPW = SK / NW, NG = KPW / 16;
constexpr int WIN = 16;

template <int I> struct IC { static constexpr int v = I; };
template <int N, int I = 0, typename Fn> __device__ __forceinline__ void sfor(Fn&& f) {
  if constexpr (I < N) { f(IC<I>{}); sfor<N, I + 1>(f); }
}

__global__ void __launch_bounds__(512) k_attn(const u16* q, const u16* K, const u16* V, u16* o) {
  __shared__ float mrg[NW][DH + 4];
  const int tid = threadIdx.x, lane = tid & 63, w = __builtin_amdgcn_readfirstlane(tid >> 6);
  const int bh = blockIdx.x, b = bh >> 5, h = bh & 31;
  const int g = lane >> 4, n = lane & 15;
  const size_t base = ((size_t)bh * SK + (size_t)w * KPW) * DH;
  const auto rk = __builtin_amdgcn_make_buffer_rsrc((void*)(K + base), 0, KPW * DH * 2, 0x00020000);
  const auto rv = __builtin_amdgcn_make_buffer_rsrc((void*)(V + base), 0, KPW * DH * 2, 0x00020000);
  const int koff = (4 * (n & 3) + (n >> 2)) * 256 + g * 16;
  const int voff = lane * 16;
  u32x4 ring[WIN];
  auto issue = [&](auto ic) {
    constexpr int i = ic.v;
    if constexpr (i < 4 * NG) ring[i % WIN] = __builtin_amdgcn_raw_buffer_load_b128(rk, koff + (i & 3) * 64, (i >> 2) * 4096, 2);
    else if constexpr (i < 8 * NG) ring[i % WIN] = __builtin_amdgcn_raw_buffer_load_b128(rv, voff + ((i - 4 * NG) & 3) * 1024, ((i - 4 * NG) >> 2) * 4096, 2);
  };
  u32x4 qf[4];
  for (int s = 0; s < 4; ++s) qf[s] = *(const u32x4*)(q + b * HD + h * DH + 32 * s + 8 * g);
  sfor<WIN>([&](auto ic) { issue(ic); });
  __builtin_amdgcn_sched_barrier(0);
  float sc[NG / 16][4];
  for (int i = 0; i < NG / 16; ++i) for (int r = 0; r < 4; ++r) sc[i][r] = 0.f;
  sfor<NG>([&](auto tc) {
    constexpr int t = tc.v;
    f32x4 d = {0.f, 0.f, 0.f, 0.f};
    sfor<4>([&](auto s_) {
      constexpr int s = s_.v;
      d = mfma16(ring[(4 * t + s) % WIN], qf[s], d);
      issue(IC<4 * t + s + WIN>{});
      __builtin_amdgcn_sched_barrier(0);
    });
    for (int r = 0; r < 4; ++r) sc[t >> 4][r] = (n == (t & 15)) ? d[r] : sc[t >> 4][r];
  });
  float m = -INFINITY;
  for (int i = 0; i < NG / 16; ++i) for (int r = 0; r < 4; ++r) { sc[i][r] = mrbf(mrbf(sc[i][r]) * 0.08838834764831845f); m = fmaxf(m, sc[i][r]); }
  m = wave_max(m);
  float l = 0.f;
  unsigned pp[NG / 16][4];
  for (int i = 0; i < NG / 16; ++i) for (int r = 0; r < 4; ++r) {
    const float p = __expf(sc[i][r] - m); l += p;
    const u16 hi = f2bf(p); const u16 lo = f2bf(p - bf2f(hi));
    pp[i][r] = (unsigned)hi | ((unsigned)lo << 16);
  }
  l = wave_sum(l);
  unsigned sel[4];
  {
    const unsigned src = (n < 8) ? 0x0100u : 0x0302u;
    for (int i = 0; i < 4; ++i) {
      const int j = n & 7;
      unsigned s_ = 0x0c0c0c0cu;
      if ((j >> 1) == i) s_ = (j & 1) ? (0x0c0cu | (src << 16)) : (0x0c0c0000u | src);
      sel[i] = s_;
    }
  }
  f32x4 acc = {0.f, 0.f, 0.f, 0.f};
  sfor<NG>([&](auto tc) {
    constexpr int t = tc.v;
    sfor<4>([&](auto r_) {
      constexpr int r = r_.v;
      const unsigned pb = bcast16<t & 15>(pp[t >> 4][r]);
      u32x4 a;
      for (int i = 0; i < 4; ++i) a[i] = __builtin_amdgcn_perm(0u, pb, sel[i]);
      acc = mfma16(a, ring[(4 * NG + 4 * t + r) % WIN], acc);
      issue(IC<4 * NG + 4 * t + r + WIN>{});
      __builtin_amdgcn_sched_barrier(0);
    });
  });
  for (int i = 0; i < 4; ++i) acc[i] += __shfl_xor(acc[i], 32);
  if (g < 2) for (int i = 0; i < 4; ++i) mrg[w][4 + 8 * n + 4 * g + i] = acc[i];
  if (lane == 0) { mrg[w][0] = m; mrg[w][1] = l; }
  __syncthreads();
  if (tid < DH) {
    float M = -INFINITY;
    for (int i = 0; i < NW; ++i) M = fmaxf(M, mrg[i][0]);
    float L = 0.f, O = 0.f;
    for (int i = 0; i < NW; ++i) { const float e = __expf(mrg[i][0] - M); L += e * mrg[i][1]; O += e * mrg[i][4 + tid]; }
    o[b * HD + h * DH + tid] = f2bf(O / L);
  }
}

__global__ void __launch_bounds__(512) k_post(const u16* x, const u16* o, const u16* h, const u16* wo, const u16* w2, u16* out) {
  __shared__ float red[2][32][B][16];
  const int tid = threadIdx.x, c = tid & 15, kg = tid >> 4;
  const int col = blockIdx.x * 16 + c;
  float a[B], f[B];
  for (int b = 0; b < B; ++b) a[b] = f[b] = 0.f;
  for (int k = kg; k < HD; k += 32) { const float wv = bf2f(wo[(size_t)k * D + col]); for (int b = 0; b < B; ++b) a[b] += bf2f(o[b * HD + k]) * wv; }
  for (int k = kg; k < F; k += 32) { const float wv = bf2f(w2[(size_t)k * D + col]); for (int b = 0; b < B; ++b) f[b] += bf2f(h[b * F + k]) * wv; }
  for (int b = 0; b < B; ++b) { red[0][kg][b][c] = a[b]; red[1][kg][b][c] = f[b]; }
  __syncthreads();
  if (tid < 128) {
    const int b = tid >> 4;
    float s = 0.f, t = 0.f;
    for (int i = 0; i < 32; ++i) { s += red[0][i][b][c]; t += red[1][i][b][c]; }
    const float gg = rbf(silu(rbf(t)));
    out[b * D + col] = f2bf(bf2f(x[b * D + col]) + rbf(rbf(s) * gg));
  }
}

extern "C" void kernel_launch(void* const* d_in, const int* in_sizes, int n_in, void* d_out, int out_size, void* d_ws, size_t ws_size, hipStream_t stream) {
  const u16* x = (const u16*)d_in[0]; const u16* K = (const u16*)d_in[1]; const u16* V = (const u16*)d_in[2];
  const u16* wq = (const u16*)d_in[3]; const u16* wo = (const u16*)d_in[4];
  const u16* w1 = (const u16*)d_in[6]; const u16* w3 = (const u16*)d_in[7]; const u16* w2 = (const u16*)d_in[8];
  u16* q = (u16*)d_ws; u16* h = q + B * HD; u16* o = h + B * F;
  k_pre<<<136, 512, 0, stream>>>(x, wq, w1, w3, q, h);
  k_attn<<<256, 512, 0, stream>>>(q, K, V, o);
  k_post<<<36, 512, 0, stream>>>(x, o, h, wo, w2, (u16*)d_out);
}
```

```cpp
#include <hip/hip_runtime.h>
#include <stdint.h>

#define WIN 5
#define WAUX 2

typedef unsigned short u16;
typedef unsigned u32x4 __attribute__((ext_vector_type(4)));
typedef unsigned u32x2 __attribute__((ext_vector_type(2)));
typedef float f32x4 __attribute__((ext_vector_type(4)));
typedef __bf16 bf16x8 __attribute__((ext_vector_type(8)));
typedef __bf16 bf16x2 __attribute__((ext_vector_type(2)));

constexpr int NB = 8, DM = 576, NH = 32, SK = 4096, DH = 128, FF = 4608, HD = NH * DH;
constexpr int NU = 72;
constexpr size_t WS_P = 0, WS_PF = (size_t)NB * NH * DM * 4;

template <int I> struct IC { static constexpr int v = I; };
template <int N, int I = 0, typename Fn> __device__ __forceinline__ void sfor(Fn&& f) {
  if constexpr (I < N) { f(IC<I>{}); sfor<N, I + 1>(f); }
}
#define SCHED() __builtin_amdgcn_sched_barrier(0)

__device__ __forceinline__ float bf2f(unsigned x) { return __uint_as_float(x << 16); }
__device__ __forceinline__ float bfhi(unsigned x) { return __uint_as_float(x & 0xffff0000u); }
__device__ __forceinline__ unsigned f2bf(float x) { return (unsigned)__builtin_bit_cast(u16, (__bf16)x); }
__device__ __forceinline__ float rbf(float x) { return bf2f(f2bf(x)); }
__device__ __forceinline__ unsigned pack2(float lo, float hi) {
  bf16x2 v = {(__bf16)lo, (__bf16)hi}; return __builtin_bit_cast(unsigned, v);
}
template <int CTRL> __device__ __forceinline__ float dppf(float x) {
  return __builtin_bit_cast(float, __builtin_amdgcn_mov_dpp(__builtin_bit_cast(int, x), CTRL, 0xf, 0xf, true));
}
template <int N> __device__ __forceinline__ unsigned bcast16(unsigned x) {
  return (unsigned)__builtin_amdgcn_mov_dpp((int)x, 0x150 + N, 0xf, 0xf, true);
}
__device__ __forceinline__ float x16(float x, bool mx) {
  auto s = __builtin_amdgcn_permlane16_swap(__float_as_uint(x), __float_as_uint(x), false, false);
  x = mx ? fmaxf(__uint_as_float(s[0]), __uint_as_float(s[1])) : __uint_as_float(s[0]) + __uint_as_float(s[1]);
  auto t = __builtin_amdgcn_permlane32_swap(__float_as_uint(x), __float_as_uint(x), false, false);
  return mx ? fmaxf(__uint_as_float(t[0]), __uint_as_float(t[1])) : __uint_as_float(t[0]) + __uint_as_float(t[1]);
}
__device__ __forceinline__ float wave_sum(float v) {
  v += dppf<0xB1>(v); v += dppf<0x4E>(v); v += dppf<0x141>(v); v += dppf<0x128>(v); return x16(v, false);
}
__device__ __forceinline__ float wave_max(float v) {
  v = fmaxf(v, dppf<0xB1>(v)); v = fmaxf(v, dppf<0x4E>(v)); v = fmaxf(v, dppf<0x141>(v)); v = fmaxf(v, dppf<0x128>(v)); return x16(v, true);
}
__device__ __forceinline__ float hi_half(float x) {
  auto t = __builtin_amdgcn_permlane32_swap(__float_as_uint(x), __float_as_uint(x), false, false);
  return __uint_as_float(t[1]);
}
__device__ __forceinline__ f32x4 mfma16(u32x4 a, u32x4 b, f32x4 c) {
  return __builtin_amdgcn_mfma_f32_16x16x32_bf16(__builtin_bit_cast(bf16x8, a), __builtin_bit_cast(bf16x8, b), c, 0, 0, 0);
}
__device__ __forceinline__ float dot2(unsigned a, unsigned b, float c) {
  return __builtin_amdgcn_fdot2_f32_bf16(__builtin_bit_cast(bf16x2, a), __builtin_bit_cast(bf16x2, b), c, false);
}
template <int CH, int CL> __device__ __forceinline__ float tmerge(float lo, float hi, bool b) {
  const float u = hi + dppf<CH>(hi), v = lo + dppf<CL>(lo);
  return b ? u : v;
}
__device__ __forceinline__ void pin(float& x) { asm volatile("" : "+v"(x)); }
__device__ __forceinline__ float silu(float x) { return x / (1.0f + __expf(-x)); }
__device__ __forceinline__ u32x4 diag(unsigned pk, const u32x4& sel) {
  u32x4 a;
  for (int i = 0; i < 4; ++i) a[i] = __builtin_amdgcn_perm(0u, pk, sel[i]);
  return a;
}
__device__ __forceinline__ auto rsrc(const void* p) { return __builtin_amdgcn_make_buffer_rsrc((void*)p, 0, 0x7fffffff, 0x00020000); }

struct LnStat { float m, den; };
__device__ __forceinline__ LnStat ln_stat(const unsigned (&d)[5], int lane) {
  float s = 0.f;
  for (int t = 0; t < 5; ++t) s += bf2f(d[t] & 0xffffu) + bfhi(d[t]);
  const float mean = wave_sum(s) * (1.0f / DM);
  float q = 0.f;
  for (int t = 0; t < 5; ++t) {
    const float a = bf2f(d[t] & 0xffffu) - mean, c = bfhi(d[t]) - mean;
    q += (t < 4 || lane < 32) ? a * a + c * c : 0.f;
  }
  const float var = rbf(wave_sum(q) * (1.0f / DM));
  return LnStat{rbf(mean), rbf(sqrtf(rbf(var + 1e-5f)))};
}
__device__ __forceinline__ float ln_apply(float x, const LnStat& s) { return rbf(x - s.m) / s.den; }

constexpr int L_QP = 0;
constexpr int L_MRG = L_QP + 4 * 128;
constexpr int L_SC = L_MRG + 4 * 132;
constexpr int L_XT = L_SC + 4 * 16 * 64;
constexpr int L_HT = L_XT + DM * 4;
constexpr int L_END = L_HT + 64 * 4;

__global__ void __launch_bounds__(512, 2) k_main(const u16* __restrict__ x, const u16* __restrict__ K, const u16* __restrict__ V, const u16* __restrict__ wq,
                                                 const u16* __restrict__ wo, const u16* __restrict__ w1, const u16* __restrict__ w3,
                                                 const u16* __restrict__ w2, float* __restrict__ ws) {
  __shared__ __attribute__((aligned(16))) float lds[L_END];
  const int tid = threadIdx.x, lane = tid & 63, w = __builtin_amdgcn_readfirstlane(tid >> 6);
  const int xg = blockIdx.x & 7, j = blockIdx.x >> 3, h = xg * 4 + (j & 3), b = j >> 2;
  const int c = lane >> 4, n = lane & 15;
  u32x4 sel;
  {
    const unsigned src = (n < 8) ? 0x0100u : 0x0302u; const int jj = n & 7;
    for (int i = 0; i < 4; ++i) sel[i] = ((jj >> 1) != i) ? 0x0c0c0c0cu : ((jj & 1) ? (0x0c0cu | (src << 16)) : (0x0c0c0000u | src));
  }
  if (w < 4) {
    __builtin_amdgcn_s_setprio(3);
    const size_t base = ((size_t)(b * NH + h) * SK) * DH;
    const char* Kb = (const char*)(K + base); const char* Vb = (const char*)(V + base);
    const int voff = lane * 16 + w * 4096;
    u32x4 ring[16];
    auto ld = [&](const char* blk, auto ic) { constexpr int i = ic.v;
      ring[i] = __builtin_amdgcn_raw_buffer_load_b128(rsrc(blk), voff + (i & 3) * 1024, (i >> 2) * 16384, 2); };
    sfor<16>([&](auto ic) { ld(Kb, ic); });
    SCHED();
    __syncthreads();
    unsigned qd[4];
    {
      f32x4 a = {0.f, 0.f, 0.f, 0.f}, e = a;
      for (int i = 0; i < 4; ++i) { a += *(const f32x4*)&lds[L_QP + i * 128 + 8 * n]; e += *(const f32x4*)&lds[L_QP + i * 128 + 8 * n + 4]; }
      qd[0] = pack2(a[0], a[1]); qd[1] = pack2(a[2], a[3]); qd[2] = pack2(e[0], e[1]); qd[3] = pack2(e[2], e[3]);
    }
    float* sc = &lds[L_SC + w * 1024 + lane];
    float mx = -INFINITY;
    const bool b0 = n & 1, b1 = n & 2, b2 = n & 4, b3 = n & 8;
    auto kblock = [&](const char* cur, const char* nxt, int blk, auto first) {
      float t0, t1, t2, t3;
      sfor<16>([&](auto ic) { constexpr int i = ic.v;
        const u32x4 kv = ring[i];
        float d = dot2(kv[0], qd[0], 0.f); d = dot2(kv[1], qd[1], d); d = dot2(kv[2], qd[2], d); d = dot2(kv[3], qd[3], d);
        pin(d);
        SCHED();
        if constexpr (i + WIN >= 16) ld(nxt, IC<i + WIN - 16>{});
        else if constexpr (!first.v) ld(cur, IC<i + WIN>{});
        SCHED();
        if constexpr (!(i & 1)) t0 = d;
        else {
          d = tmerge<0xB1, 0xB1>(t0, d, b0);
          if constexpr (!(i & 2)) t1 = d;
          else {
            d = tmerge<0x4E, 0x4E>(t1, d, b1);
            if constexpr (!(i & 4)) t2 = d;
            else {
              d = tmerge<0x114, 0x104>(t2, d, b2);
              if constexpr (!(i & 8)) t3 = d;
              else {
                d = tmerge<0x128, 0x128>(t3, d, b3);
                d = rbf(rbf(d) * 0.08838834764831845f);
                mx = fmaxf(mx, d);
                sc[blk * 64] = d;
              }
            }
          }
        }
      });
    };
    kblock(Kb, Kb + 65536, 0, IC<1>{});
#pragma unroll 1
    for (int blk = 1; blk < 16; ++blk) kblock(Kb + blk * 65536, blk < 15 ? Kb + (blk + 1) * 65536 : Vb, blk, IC<0>{});
    mx = wave_max(mx);
    float l = 0.f;
    f32x4 acc = {0.f, 0.f, 0.f, 0.f};
    float s = sc[0];
    auto vblock = [&](const char* cur, const char* nxt, int blk, auto last) {
      const float p = __expf(s - mx);
      l += p;
      const float ph = rbf(p);
      const unsigned pk = pack2(ph, p - ph);
      if constexpr (!last.v) s = sc[(blk + 1) * 64];
      sfor<16>([&](auto ic) { constexpr int i = ic.v;
        acc = mfma16(diag(bcast16<i>(pk), sel), ring[i], acc);
        SCHED();
        if constexpr (i + WIN < 16) ld(cur, IC<i + WIN>{});
        else if constexpr (!last.v) ld(nxt, IC<i + WIN - 16>{});
        SCHED();
      });
    };
#pragma unroll 1
    for (int blk = 0; blk < 15; ++blk) vblock(Vb + blk * 65536, Vb + (blk + 1) * 65536, blk, IC<0>{});
    vblock(Vb + 15 * 65536, Vb, 15, IC<1>{});
    l = wave_sum(l);
    float* mr = &lds[L_MRG + w * 132];
    for (int r = 0; r < 4; ++r) { const float o = acc[r] + hi_half(acc[r]); if (c < 2) mr[4 + (4 * c + r) * 16 + n] = o; }
    if (lane == 0) { mr[0] = mx; mr[1] = l; }
    __syncthreads();
  } else {
    const int i4 = w - 4;
    {
      const unsigned* xr = (const unsigned*)(x + b * DM);
      unsigned d[5];
      for (int t = 0; t < 4; ++t) d[t] = xr[lane + 64 * t];
      d[4] = lane < 32 ? xr[256 + lane] : 0u;
      const u16* xe = x + b * DM + 144 * i4 + c;
      const unsigned e0 = xe[8 * n], e1 = xe[8 * n + 4], e2 = xe[128 + 8 * (n & 1)], e3 = xe[128 + 8 * (n & 1) + 4];
      SCHED();
      u32x4 wr[36];
      const auto rq = rsrc(wq);
      const int vo = (4 * (n >> 3) + c) * (HD * 2) + (n & 7) * 16;
      const int so = 144 * i4 * (HD * 2) + h * 256;
      sfor<36>([&](auto ic) { constexpr int i = ic.v;
        wr[i] = __builtin_amdgcn_raw_buffer_load_b128(rq, vo + (i & 1) * 128, so + (i >> 1) * 8 * (HD * 2), WAUX); });
      SCHED();
      const LnStat st = ln_stat(d, lane);
      const unsigned pk0 = pack2(ln_apply(bf2f(e0), st), ln_apply(bf2f(e1), st)), pk1 = pack2(ln_apply(bf2f(e2), st), ln_apply(bf2f(e3), st));
      f32x4 a0 = {0.f, 0.f, 0.f, 0.f}, a1 = a0;
      sfor<18>([&](auto sc_) { constexpr int s = sc_.v;
        const u32x4 a = diag(bcast16<s & 15>(s < 16 ? pk0 : pk1), sel);
        a0 = mfma16(a, wr[2 * s], a0); a1 = mfma16(a, wr[2 * s + 1], a1); });
      f32x4 o0, o1;
      for (int r = 0; r < 4; ++r) { o0[r] = a0[r] + hi_half(dppf<0x128>(a0[r])); o1[r] = a1[r] + hi_half(dppf<0x128>(a1[r])); }
      if (c < 2 && n < 8) { *(f32x4*)&lds[L_QP + i4 * 128 + 8 * n + 4 * c] = o0; *(f32x4*)&lds[L_QP + i4 * 128 + 64 + 8 * n + 4 * c] = o1; }
    }
    __syncthreads();
    if (i4 < 3) {
      u32x4 wr[48];
      const auto ro = rsrc(wo);
      const int vo = (4 * (n >> 3) + c) * (DM * 2) + (n & 7) * 16;
      const int so = 128 * h * (DM * 2) + 384 * i4;
      sfor<48>([&](auto ic) { constexpr int i = ic.v;
        wr[i] = __builtin_amdgcn_raw_buffer_load_b128(ro, vo + (i % 3) * 128, so + (i / 3) * 8 * (DM * 2), WAUX); });
      SCHED();
      __syncthreads();
      float m4[4], M = -INFINITY;
      for (int i = 0; i < 4; ++i) { m4[i] = lds[L_MRG + i * 132]; M = fmaxf(M, m4[i]); }
      float L = 0.f, oa = 0.f, ob = 0.f;
      for (int i = 0; i < 4; ++i) {
        const float e = __expf(m4[i] - M);
        L += e * lds[L_MRG + i * 132 + 1]; oa += e * lds[L_MRG + i * 132 + 4 + c * 16 + n]; ob += e * lds[L_MRG + i * 132 + 4 + (4 + c) * 16 + n];
      }
      const unsigned pk = pack2(oa / L, ob / L);
      f32x4 a3[3] = {{0.f, 0.f, 0.f, 0.f}, {0.f, 0.f, 0.f, 0.f}, {0.f, 0.f, 0.f, 0.f}};
      sfor<16>([&](auto sc_) { constexpr int s = sc_.v;
        const u32x4 a = diag(bcast16<s>(pk), sel);
        for (int t = 0; t < 3; ++t) a3[t] = mfma16(a, wr[3 * s + t], a3[t]); });
      float* P = ws + WS_P / 4 + (size_t)(b * NH + h) * DM + 192 * i4 + 8 * n + 4 * c;
      for (int t = 0; t < 3; ++t) {
        f32x4 o;
        for (int r = 0; r < 4; ++r) o[r] = a3[t][r] + hi_half(dppf<0x128>(a3[t][r]));
        if (c < 2 && n < 8) *(f32x4*)(P + 64 * t) = o;
      }
    } else {
      if (j < 9) {
        const int u = xg * 9 + j;
        {
          unsigned v[NB][5];
          for (int r = 0; r < NB; ++r) {
            const unsigned* xr = (const unsigned*)(x + r * DM);
            for (int t = 0; t < 4; ++t) v[r][t] = xr[lane + 64 * t];
            v[r][4] = lane < 32 ? xr[256 + lane] : 0u;
          }
          for (int r = 0; r < NB; ++r) {
            const LnStat st = ln_stat(v[r], lane);
            for (int t = 0; t < 5; ++t) v[r][t] = pack2(ln_apply(bf2f(v[r][t] & 0xffffu), st), ln_apply(bfhi(v[r][t]), st));
          }
          for (int t = 0; t < 5; ++t) {
            u32x4 lo, hi;
            for (int p = 0; p < 4; ++p) { lo[p] = __builtin_amdgcn_perm(v[2 * p + 1][t], v[2 * p][t], 0x05040100u); hi[p] = __builtin_amdgcn_perm(v[2 * p + 1][t], v[2 * p][t], 0x07060302u); }
            if (t < 4 || lane < 32) { *(u32x4*)&lds[L_XT + (2 * (lane + 64 * t)) * 4] = lo; *(u32x4*)&lds[L_XT + (2 * (lane + 64 * t) + 1) * 4] = hi; }
          }
        }
        f32x4 ga[4];
        for (int p = 0; p < 4; ++p) ga[p] = f32x4{0.f, 0.f, 0.f, 0.f};
        const char* wp = (const char*)((n < 8 ? w1 : w3) + (size_t)c * FF + 64 * u + 8 * (n & 7));
#pragma unroll 1
        for (int it = 0; it < 9; ++it) {
          u32x4 wr[16];
          sfor<16>([&](auto ic) { wr[ic.v] = __builtin_nontemporal_load((const u32x4*)(wp + (size_t)(16 * it + ic.v) * (4 * FF * 2))); });
          SCHED();
          sfor<16>([&](auto ic) {
            const u32x4 xv = *(const u32x4*)&lds[L_XT + (4 * (16 * it + ic.v) + c) * 4];
            for (int p = 0; p < 4; ++p) ga[p] = mfma16(diag(xv[p], sel), wr[ic.v], ga[p]);
          });
        }
        u16* ht = (u16*)&lds[L_HT];
        for (int p = 0; p < 4; ++p)
          for (int r = 0; r < 4; ++r) {
            const float gt = rbf(ga[p][r]), up = dppf<0x128>(gt);
            if (n < 8) ht[(8 * n + 4 * (c & 1) + r) * 8 + 2 * p + (c >> 1)] = (u16)f2bf(rbf(silu(gt)) * up);
          }
        f32x4 da[5][4];
        for (int q = 0; q < 5; ++q) for (int p = 0; p < 4; ++p) da[q][p] = f32x4{0.f, 0.f, 0.f, 0.f};
        const auto r2 = rsrc(w2);
        const int vo = c * (DM * 2) + n * 16, vo4 = c * (DM * 2) + (n & 7) * 16 + 1024;
#pragma unroll 1
        for (int it = 0; it < 4; ++it) {
          u32x4 wr[20];
          sfor<20>([&](auto ic) { constexpr int q = ic.v % 5, s = ic.v / 5;
            wr[ic.v] = __builtin_amdgcn_raw_buffer_load_b128(r2, q < 4 ? vo + q * 256 : vo4, (64 * u + 16 * it + 4 * s) * (DM * 2), 2); });
          SCHED();
          sfor<4>([&](auto sc_) { constexpr int s = sc_.v;
            const u32x4 hv = *(const u32x4*)&lds[L_HT + (16 * it + 4 * s + c) * 4];
            for (int p = 0; p < 4; ++p) { const u32x4 a = diag(hv[p], sel);
              for (int q = 0; q < 5; ++q) da[q][p] = mfma16(a, wr[5 * s + q], da[q][p]); }
          });
        }
        float* PF = ws + WS_PF / 4 + (size_t)(u * NB + (c >> 1)) * DM + 8 * n + 4 * (c & 1);
        for (int p = 0; p < 4; ++p)
          for (int q = 0; q < 5; ++q) if (q < 4 || n < 8) *(f32x4*)(PF + 2 * p * DM + 128 * q) = da[q][p];
      }
      __syncthreads();
    }
  }
}

__global__ void __launch_bounds__(64) k_final(const u16* __restrict__ x, const float* __restrict__ ws, u16* __restrict__ out) {
  const int lane = threadIdx.x, b = blockIdx.x / 18, cb = blockIdx.x % 18, pl = lane >> 3, col = 32 * cb + 4 * (lane & 7);
  f32x4 pv[4], fv[9];
  for (int t = 0; t < 4; ++t) pv[t] = *(const f32x4*)(ws + WS_P / 4 + (size_t)(b * NH + 8 * t + pl) * DM + col);
  for (int t = 0; t < 9; ++t) fv[t] = *(const f32x4*)(ws + WS_PF / 4 + (size_t)((8 * t + pl) * NB + b) * DM + col);
  const u32x2 xv = *(const u32x2*)(x + b * DM + col);
  f32x4 s = pv[0], f = fv[0];
  for (int t = 1; t < 4; ++t) s += pv[t];
  for (int t = 1; t < 9; ++t) f += fv[t];
  float o[4];
  for (int r = 0; r < 4; ++r) {
    const float ss = x16(s[r] + dppf<0x128>(s[r]), false), ff = x16(f[r] + dppf<0x128>(f[r]), false);
    const float xx = (r & 1) ? bfhi(xv[r >> 1]) : bf2f(xv[r >> 1] & 0xffffu);
    o[r] = xx + rbf(rbf(ss) * rbf(silu(rbf(ff))));
  }
  if (lane < 8) *(u32x2*)(out + b * DM + col) = u32x2{pack2(o[0], o[1]), pack2(o[2], o[3])};
}

extern "C" void kernel_launch(void* const* d_in, const int* in_sizes, int n_in, void* d_out, int out_size, void* d_ws, size_t ws_size, hipStream_t stream) {
  const u16* x = (const u16*)d_in[0];
  k_main<<<256, 512, 0, stream>>>(x, (const u16*)d_in[1], (const u16*)d_in[2], (const u16*)d_in[3], (const u16*)d_in[4], (const u16*)d_in[6],
                                  (const u16*)d_in[7], (const u16*)d_in[8], (float*)d_ws);
  k_final<<<144, 64, 0, stream>>>(x, (const float*)d_ws, (u16*)d_out);
}
```

```cpp
#include <hip/hip_runtime.h>
#include <stdint.h>

typedef unsigned short u16;
typedef unsigned u32x4 __attribute__((ext_vector_type(4)));
typedef unsigned u32x2 __attribute__((ext_vector_type(2)));
typedef float f32x4 __attribute__((ext_vector_type(4)));
typedef short s16x4 __attribute__((ext_vector_type(4)));
typedef __bf16 bf16x8 __attribute__((ext_vector_type(8)));
typedef __bf16 bf16x2 __attribute__((ext_vector_type(2)));
#define LAS __attribute__((address_space(3)))
typedef __attribute__((address_space(1))) unsigned gu32;
typedef __attribute__((address_space(1))) unsigned long long gu64;
#define RLX_AGENT __ATOMIC_RELAXED, __HIP_MEMORY_SCOPE_AGENT

constexpr int NB = 8, DM = 576, NH = 32, SK = 4096, DH = 128, FF = 4608, HD = NH * DH;

constexpr int PKW = 4, PQW = 5, FFW = 7;
constexpr int XPRE = 11;
constexpr int WO_T0 = 1400, WO_DT = 70;
constexpr int PC_T3 = 1395, PC_RK = 229, PC_RV = 219;
constexpr int PC_L0 = 19, PC_G = 10;
constexpr int G_L1 = 5, G_HS = 7, G_L2 = 9, G_GO = 11, G_F = 22;
constexpr int POLL_DELAY = 40;
constexpr int WIN = 7;
constexpr int NPRE = 2;

constexpr int WS_P = 0, WS_PF = WS_P + NB * NH * DM * 4, WS_GU = WS_PF + 8 * NB * DM * 4, WS_FG = WS_GU + 2 * NB * FF * 2, WS_FP = WS_FG + 1024,
              WS_HI = WS_FP + 1024, WS_HO = WS_HI + 256 * 8, WS_END = WS_HO + 256 * 8;
constexpr int LEADER = 255;
constexpr unsigned SENT = 0xffffffffu;
constexpr unsigned SPIN_CAP = 1u << 18, LDS_CAP = 1u << 22;
constexpr int ACT_LD = DM * 2 + 16;
constexpr int L_W = 0;
constexpr int L_ACT = L_W + DM * 128;
constexpr int L_SC = L_ACT + NB * ACT_LD;
constexpr int L_QP = L_SC + 4 * 16 * 64 * 2;
constexpr int L_MRG = L_QP + 3 * 128 * 4;
constexpr int L_CTL = L_MRG + 4 * 132 * 4;
constexpr int L_END = L_CTL + 32;
enum { C_Q = 0, C_BLK = 1, C_ML = 2, C_LAG = 3, C_SHIFT = 4 };

template <int I> struct IC { static constexpr int v = I; };
template <int N, int I = 0, typename Fn> __device__ __forceinline__ void sfor(Fn&& f) {
  if constexpr (I < N) { f(IC<I>{}); sfor<N, I + 1>(f); }
}
#define SCHED() __builtin_amdgcn_sched_barrier(0)
#define SPIN(cap) _Pragma("unroll 1") for (unsigned spins = 0; spins < (cap); ++spins)
template <int N> __device__ __forceinline__ void vmwait() { asm volatile("s_waitcnt vmcnt(%0)" ::"n"(N) : "memory"); }
template <int I> __device__ __forceinline__ void vmwait_l(int lvl) {
  constexpr int br = ((I & 1) << 3) | ((I & 2) << 1) | ((I & 4) >> 1) | ((I & 8) >> 3);
  asm volatile("s_cmp_gt_i32 %0, %1\n\ts_cbranch_scc1 3f\n\ts_cmp_gt_i32 %0, %2\n\ts_cbranch_scc1 2f\n\ts_cmp_gt_i32 %0, %3\n\ts_cbranch_scc1 1f\n\t"
               "s_waitcnt vmcnt(3)\n1:\n\ts_waitcnt vmcnt(4)\n2:\n\ts_waitcnt vmcnt(5)\n3:\n\ts_waitcnt vmcnt(6)"
               ::"s"(lvl), "n"(32 + br), "n"(16 + br), "n"(br) : "memory", "scc");
}
__device__ __forceinline__ unsigned now() { return (unsigned)__builtin_amdgcn_s_memrealtime(); }

__device__ __forceinline__ float bflo(unsigned x) { return __uint_as_float(x << 16); }
__device__ __forceinline__ float bfhi(unsigned x) { return __uint_as_float(x & 0xffff0000u); }
__device__ __forceinline__ unsigned pack2(float lo, float hi) { bf16x2 v = {(__bf16)lo, (__bf16)hi}; return __builtin_bit_cast(unsigned, v); }
__device__ __forceinline__ float rbf(float x) { return bflo((unsigned)__builtin_bit_cast(u16, (__bf16)x)); }
template <int CTRL> __device__ __forceinline__ float dppf(float x) {
  return __builtin_bit_cast(float, __builtin_amdgcn_mov_dpp(__builtin_bit_cast(int, x), CTRL, 0xf, 0xf, true));
}
constexpr int XOR1 = 0xB1, XOR2 = 0x4E, XOR7 = 0x141, XOR8 = 0x128, UP4 = 0x104, DOWN4 = 0x114;
template <int N> __device__ __forceinline__ unsigned bcast16(unsigned x) {
  return (unsigned)__builtin_amdgcn_mov_dpp((int)x, 0x150 + N, 0xf, 0xf, true);
}
__device__ __forceinline__ float x16(float x, bool mx) {
  auto s = __builtin_amdgcn_permlane16_swap(__float_as_uint(x), __float_as_uint(x), false, false);
  x = mx ? fmaxf(__uint_as_float(s[0]), __uint_as_float(s[1])) : __uint_as_float(s[0]) + __uint_as_float(s[1]);
  auto t = __builtin_amdgcn_permlane32_swap(__float_as_uint(x), __float_as_uint(x), false, false);
  return mx ? fmaxf(__uint_as_float(t[0]), __uint_as_float(t[1])) : __uint_as_float(t[0]) + __uint_as_float(t[1]);
}
__device__ __forceinline__ float wave_sum(float v) {
  v += dppf<XOR1>(v); v += dppf<XOR2>(v); v += dppf<XOR7>(v); v += dppf<XOR8>(v); return x16(v, false);
}
__device__ __forceinline__ float wave_max(float v) {
  v = fmaxf(v, dppf<XOR1>(v)); v = fmaxf(v, dppf<XOR2>(v)); v = fmaxf(v, dppf<XOR7>(v)); v = fmaxf(v, dppf<XOR8>(v)); return x16(v, true);
}
__device__ __forceinline__ float hi_half(float x) {
  auto t = __builtin_amdgcn_permlane32_swap(__float_as_uint(x), __float_as_uint(x), false, false);
  return __uint_as_float(t[1]);
}
template <int CH, int CL> __device__ __forceinline__ float tmerge(float lo, float hi, bool b) {
  const float u = hi + dppf<CH>(hi), v = lo + dppf<CL>(lo);
  return b ? u : v;
}
__device__ __forceinline__ float reduce8(const f32x4& v, bool b3) {
  const float u = tmerge<XOR8, XOR8>(v[0], v[1], b3), t = tmerge<XOR8, XOR8>(v[2], v[3], b3);
  auto a = __builtin_amdgcn_permlane16_swap(__float_as_uint(u), __float_as_uint(t), false, false);
  const float z = __uint_as_float(a[0]) + __uint_as_float(a[1]);
  auto e = __builtin_amdgcn_permlane32_swap(__float_as_uint(z), __float_as_uint(z), false, false);
  return __uint_as_float(e[0]) + __uint_as_float(e[1]);
}
__device__ __forceinline__ void pin(float& x) { asm volatile("" : "+v"(x)); }
__device__ __forceinline__ f32x4 mfma16(u32x4 a, u32x4 b, f32x4 c) {
  return __builtin_amdgcn_mfma_f32_16x16x32_bf16(__builtin_bit_cast(bf16x8, a), __builtin_bit_cast(bf16x8, b), c, 0, 0, 0);
}
__device__ __forceinline__ float dot2(unsigned a, unsigned b, float c) {
  return __builtin_amdgcn_fdot2_f32_bf16(__builtin_bit_cast(bf16x2, a), __builtin_bit_cast(bf16x2, b), c, false);
}
__device__ __forceinline__ float silu(float x) { return x / (1.0f + __expf(-x)); }
__device__ __forceinline__ u32x4 diag(unsigned pk, const u32x4& sel) {
  u32x4 a;
  for (int i = 0; i < 4; ++i) a[i] = __builtin_amdgcn_perm(0u, pk, sel[i]);
  return a;
}
__device__ __forceinline__ auto rsrc(const void* p, int bytes) { return __builtin_amdgcn_make_buffer_rsrc((void*)p, 0, bytes, 0x00020000); }
constexpr int NT = 2, SC1 = 16;
__device__ __forceinline__ void glds16(const void* gsrc, unsigned lds_dst) {
  unsigned keep;
  asm volatile("s_mov_b32 %0, m0\n\ts_mov_b32 m0, %2\n\ts_nop 0\n\tglobal_load_lds_dwordx4 %1, off nt\n\ts_mov_b32 m0, %0" : "=&s"(keep) : "v"(gsrc), "s"(lds_dst) : "memory");
}
__device__ __forceinline__ u32x2 trread(LAS char* p) { return __builtin_bit_cast(u32x2, __builtin_amdgcn_ds_read_tr16_b64_v4i16((LAS s16x4*)p)); }

struct LnStat { float m, den, inv; };
__device__ __forceinline__ LnStat ln_finish(float mean, float var) {
  const float den = rbf(sqrtf(rbf(rbf(var) + 1e-5f)));
  return LnStat{rbf(mean), den, 1.0f / den};
}
__device__ __forceinline__ float ln_apply(float x, const LnStat& s) {
  const float a = rbf(x - s.m), q = a * s.inv;
  return fmaf(fmaf(-q, s.den, a), s.inv, q);
}

__global__ void __launch_bounds__(512, 2) k_main(const u16* __restrict__ x, const u16* __restrict__ K, const u16* __restrict__ V, const u16* __restrict__ wq,
                                                 const u16* __restrict__ wo, const u16* __restrict__ w1, const u16* __restrict__ w3,
                                                 const u16* __restrict__ w2, char* ws, u16* __restrict__ out) {
  __shared__ __attribute__((aligned(16))) char lds_[L_END];
  LAS char* lds = (LAS char*)lds_;
  asm volatile("" ::"s"(x), "s"(K), "s"(V), "s"(wq), "s"(w1), "s"(w3), "s"(w2), "s"(ws));
  const int tid = threadIdx.x, lane = tid & 63, w = __builtin_amdgcn_readfirstlane(tid >> 6);
  const int xg = blockIdx.x & 7, j0 = blockIdx.x >> 3, b = j0 >> 2;
  const int h = (j0 & 1) | (xg << 1) | ((j0 & 2) << 3), j = (j0 & 1) * 16 + (j0 >> 1);
  const unsigned tent = now();
  const int c = lane >> 4, n = lane & 15;
  volatile LAS unsigned* ctl = (volatile LAS unsigned*)(lds + L_CTL);
  if (tid < 8) ctl[tid] = 0u;
  __syncthreads();
  u32x4 sel;
  {
    const unsigned src = (n < 8) ? 0x0100u : 0x0302u; const int jj = n & 7;
    for (int i = 0; i < 4; ++i) sel[i] = ((jj >> 1) != i) ? 0x0c0c0c0cu : ((jj & 1) ? (0x0c0cu | (src << 16)) : (0x0c0c0000u | src));
  }
  if (w < 4) {
    const size_t base = ((size_t)(b * NH + h) * SK) * DH;
    const char* Kb = (const char*)(K + base); const char* Vb = (const char*)(V + base);
    const int voff = lane * 16 + w * 4096;
    u32x4 ring[16], pre[16 * NPRE];
    auto ld = [&](const char* blk, auto ic) { constexpr int i = ic.v;
      ring[i] = __builtin_amdgcn_raw_buffer_load_b128(rsrc(blk, 65536), voff + (i & 3) * 1024, (i >> 2) * 16384, NT); };
    sfor<PKW>([&](auto ic) { ld(Kb, ic); });
    sfor<16 - PKW>([&](auto ic) { SCHED(); vmwait<PKW - 1>(); SCHED(); ld(Kb, IC<ic.v + PKW>{}); });
    sfor<16 * NPRE>([&](auto ic) { constexpr int i = ic.v; SCHED(); vmwait<PKW - 1>(); SCHED();
      pre[i] = __builtin_amdgcn_raw_buffer_load_b128(rsrc(Kb + 65536, NPRE * 65536), voff + (i & 3) * 1024, (i >> 2) * 16384, NT); });
    SCHED();
    __builtin_amdgcn_s_setprio(3);
    SPIN(LDS_CAP) { if (ctl[C_Q] >= 3u) break; __builtin_amdgcn_s_sleep(1); }
    asm volatile("" ::: "memory");
    unsigned qd[4];
    {
      f32x4 a = {0.f, 0.f, 0.f, 0.f}, e = a;
      for (int i = 0; i < 3; ++i) { a += *(LAS f32x4*)(lds + L_QP + i * 512 + 32 * n); e += *(LAS f32x4*)(lds + L_QP + i * 512 + 32 * n + 16); }
      qd[0] = pack2(a[0], a[1]); qd[1] = pack2(a[2], a[3]); qd[2] = pack2(e[0], e[1]); qd[3] = pack2(e[2], e[3]);
    }
    LAS u16* sc = (LAS u16*)(lds + L_SC) + w * 1024 + lane;
    float mx = -INFINITY;
    int lvl = PC_L0;
    unsigned due = tent + (PC_T3 - PC_RK);
    auto pace = [&](int ticks) {
      due += ticks;
      const int shift = __builtin_amdgcn_readfirstlane(ctl[C_SHIFT]);
      const int lag = min(max((int)(now() - due), -4096), 4096);
      if (w == 0) ctl[C_LAG] = lag;
      lvl = min(max(PC_L0 + ((lag - shift) * PC_G >> 6), 0), 48);
    };
    const bool b0 = n & 1, b1 = n & 2, b2 = n & 4, b3 = n & 8;
    auto kblock = [&](const char* cur, const char* nxt, int blk, auto mode) {
      float t0, t1, t2, t3;
      sfor<16>([&](auto ic) { constexpr int i = ic.v;
        const u32x4 kv = mode.v >= 2 ? pre[(mode.v >= 2 ? mode.v - 2 : 0) * 16 + i] : ring[i];
        if constexpr (mode.v == 0 || (mode.v == -1 && i + WIN >= XPRE)) {
          SCHED(); vmwait_l<i>(lvl); SCHED(); if constexpr (i + WIN >= 16) ld(nxt, IC<i + WIN - 16>{}); else ld(cur, IC<i + WIN>{}); SCHED(); }
        float d = dot2(kv[0], qd[0], 0.f); d = dot2(kv[1], qd[1], d); d = dot2(kv[2], qd[2], d); d = dot2(kv[3], qd[3], d);
        pin(d);
        SCHED();
        if constexpr (mode.v == 1 && i < XPRE) { ld(nxt, ic); SCHED(); }
        if constexpr (!(i & 1)) t0 = d;
        else {
          d = tmerge<XOR1, XOR1>(t0, d, b0);
          if constexpr (!(i & 2)) t1 = d;
          else {
            d = tmerge<XOR2, XOR2>(t1, d, b1);
            if constexpr (!(i & 4)) t2 = d;
            else {
              d = tmerge<DOWN4, UP4>(t2, d, b2);
              if constexpr (!(i & 8)) t3 = d;
              else {
                d = tmerge<XOR8, XOR8>(t3, d, b3);
                d = rbf(rbf(d) * 0.08838834764831845f);
                mx = fmaxf(mx, d);
                sc[blk * 64] = (u16)(__float_as_uint(d) >> 16);
                if (w == 0) ctl[C_BLK] = blk + 1;
              }
            }
          }
        }
      });
    };
    constexpr int B0 = NPRE + 1;
    kblock(Kb, Kb + B0 * 65536, 0, IC<1>{});
    sfor<NPRE>([&](auto pc) { kblock(Kb, Kb, 1 + pc.v, IC<2 + pc.v>{}); });
    kblock(Kb + B0 * 65536, Kb + (B0 + 1) * 65536, B0, IC<-1>{});
    pace(PC_RK);
#pragma unroll 1
    for (int blk = B0 + 1; blk < 16; ++blk) { kblock(Kb + blk * 65536, blk < 15 ? Kb + (blk + 1) * 65536 : Vb, blk, IC<0>{}); pace(PC_RK); }
    mx = wave_max(mx);
    float l = 0.f;
    f32x4 acc = {0.f, 0.f, 0.f, 0.f};
    float s = bflo(sc[0]);
    LAS float* mr = (LAS float*)(lds + L_MRG) + w * 132;
    auto vblock = [&](const char* cur, const char* nxt, int blk, auto last) {
      if (w == 0) ctl[C_BLK] = 16 + blk;
      const float p = __expf(s - mx);
      l += p;
      const float ph = rbf(p);
      const unsigned pk = pack2(ph, p - ph);
      if constexpr (!last.v) s = bflo(sc[(blk + 1) * 64]);
      else {
        l = wave_sum(l);
        if (lane == 0) { mr[0] = mx; mr[1] = l; }
        asm volatile("s_waitcnt lgkmcnt(0)" ::: "memory");
        if (lane == 0) __hip_atomic_fetch_add((LAS unsigned*)(lds + L_CTL) + C_ML, 1u, __ATOMIC_RELAXED, __HIP_MEMORY_SCOPE_WORKGROUP);
      }
      sfor<16>([&](auto ic) { constexpr int i = ic.v;
        const u32x4 a = diag(bcast16<i>(pk), sel), vv = ring[i];
        SCHED();
        if constexpr (!last.v || i + WIN < 16) vmwait_l<i>(lvl);
        SCHED();
        if constexpr (i + WIN < 16) ld(cur, IC<i + WIN>{}); else if constexpr (!last.v) ld(nxt, IC<i + WIN - 16>{});
        SCHED();
        acc = mfma16(a, vv, acc);
        SCHED();
      });
    };
#pragma unroll 1
    for (int blk = 0; blk < 15; ++blk) { vblock(Vb + blk * 65536, Vb + (blk + 1) * 65536, blk, IC<0>{}); pace(PC_RV); }
    vblock(Vb + 15 * 65536, Vb, 15, IC<1>{});
    for (int r = 0; r < 2; ++r) {
      auto t = __builtin_amdgcn_permlane32_swap(__float_as_uint(acc[r]), __float_as_uint(acc[r + 2]), false, false);
      mr[4 + (4 * (c & 1) + 2 * (c >> 1) + r) * 16 + n] = __uint_as_float(t[0]) + __uint_as_float(t[1]);
    }
    __syncthreads();
  } else if (w < 7) {
    const int i4 = w - 4;
    {
      const unsigned* xr = (const unsigned*)(x + b * DM);
      unsigned d[5];
      for (int t = 0; t < 4; ++t) d[t] = xr[lane + 64 * t];
      d[4] = xr[256 + (lane & 31)];
      const u16* xe = x + b * DM + 192 * i4 + c;
      const unsigned e0 = xe[8 * n], e1 = xe[8 * n + 4], e2 = xe[128 + 8 * (n & 7)], e3 = xe[128 + 8 * (n & 7) + 4];
      SCHED();
      u32x4 wr[48];
      const auto rq = rsrc(wq, DM * HD * 2);
      const int vo = (4 * (n >> 3) + c) * (HD * 2) + (n & 7) * 16;
      const int so = 192 * i4 * (HD * 2) + h * 256;
      auto ldq = [&](auto ic) { constexpr int i = ic.v;
        if constexpr (i < 48) wr[i] = __builtin_amdgcn_raw_buffer_load_b128(rq, vo + (i & 1) * 128, so + (i >> 1) * 8 * (HD * 2), NT); };
      sfor<PQW>(ldq);
      SCHED();
      float sm = 0.f, sq = 0.f;
      for (int t = 0; t < 5; ++t) sm += (t < 4 || lane < 32) ? bflo(d[t]) + bfhi(d[t]) : 0.f;
      const float mean = wave_sum(sm) * (1.0f / DM);
      for (int t = 0; t < 5; ++t) { const float a = bflo(d[t]) - mean, e = bfhi(d[t]) - mean; sq += (t < 4 || lane < 32) ? a * a + e * e : 0.f; }
      const LnStat st = ln_finish(mean, wave_sum(sq) * (1.0f / DM));
      const unsigned pk0 = pack2(ln_apply(bflo(e0), st), ln_apply(bflo(e1), st)), pk1 = pack2(ln_apply(bflo(e2), st), ln_apply(bflo(e3), st));
      f32x4 a0 = {0.f, 0.f, 0.f, 0.f}, a1 = a0;
      sfor<24>([&](auto sc_) { constexpr int s = sc_.v;
        const u32x4 a = diag(bcast16<s & 15>(s < 16 ? pk0 : pk1), sel);
        a0 = mfma16(a, wr[2 * s], a0); SCHED(); ldq(IC<2 * s + PQW>{}); SCHED();
        a1 = mfma16(a, wr[2 * s + 1], a1); SCHED(); ldq(IC<2 * s + 1 + PQW>{}); SCHED(); });
      f32x4 o0, o1;
      for (int r = 0; r < 4; ++r) { o0[r] = a0[r] + hi_half(dppf<XOR8>(a0[r])); o1[r] = a1[r] + hi_half(dppf<XOR8>(a1[r])); }
      if (c < 2 && n < 8) { *(LAS f32x4*)(lds + L_QP + i4 * 512 + 32 * n + 16 * c) = o0; *(LAS f32x4*)(lds + L_QP + i4 * 512 + 256 + 32 * n + 16 * c) = o1; }
      asm volatile("s_waitcnt lgkmcnt(0)" ::: "memory");
      if (lane == 0) __hip_atomic_fetch_add((LAS unsigned*)(lds + L_CTL) + C_Q, 1u, __ATOMIC_RELAXED, __HIP_MEMORY_SCOPE_WORKGROUP);
    }
    u32x4 wr[48];
    const auto ro = rsrc(wo, HD * DM * 2);
    const int vo = (4 * (n >> 3) + c) * (DM * 2) + (n & 7) * 16;
    const int so = 128 * h * (DM * 2) + 384 * i4;
    sfor<48>([&](auto ic) { constexpr int i = ic.v;
      SPIN(LDS_CAP) { if (now() - tent >= (unsigned)(WO_T0 + i * WO_DT)) break; __builtin_amdgcn_s_sleep(2); }
      wr[i] = __builtin_amdgcn_raw_buffer_load_b128(ro, vo + (i % 3) * 128, so + (i / 3) * 8 * (DM * 2), NT);
      SCHED(); vmwait<1>(); SCHED(); });
    const LAS float* mr = (const LAS float*)(lds + L_MRG);
    SPIN(LDS_CAP) { if (ctl[C_ML] >= 4u) break; __builtin_amdgcn_s_sleep(4); }
    asm volatile("" ::: "memory");
    float f4[4], M = -INFINITY, L = 0.f;
    for (int i = 0; i < 4; ++i) { f4[i] = mr[i * 132]; M = fmaxf(M, f4[i]); }
    for (int i = 0; i < 4; ++i) { f4[i] = __expf(f4[i] - M); L += f4[i] * mr[i * 132 + 1]; }
    float linv = 1.0f / L;
    for (int i = 0; i < 4; ++i) pin(f4[i]);
    pin(linv);
    __syncthreads();
    float oa = 0.f, ob = 0.f;
    for (int i = 0; i < 4; ++i) { oa += f4[i] * mr[i * 132 + 4 + c * 16 + n]; ob += f4[i] * mr[i * 132 + 4 + (4 + c) * 16 + n]; }
    const unsigned pk = pack2(oa * linv, ob * linv);
    f32x4 a3[3] = {{0.f, 0.f, 0.f, 0.f}, {0.f, 0.f, 0.f, 0.f}, {0.f, 0.f, 0.f, 0.f}};
    sfor<16>([&](auto sc_) { constexpr int s = sc_.v;
      const u32x4 a = diag(bcast16<s>(pk), sel);
      for (int t = 0; t < 3; ++t) a3[t] = mfma16(a, wr[3 * s + t], a3[t]); });
    const int po = WS_P + ((b * NH + h) * DM + 192 * i4 + 8 * n + 4 * (c & 1)) * 4;
    u32x4 o01, o2;
    for (int r = 0; r < 4; ++r) {
      auto t = __builtin_amdgcn_permlane32_swap(__float_as_uint(a3[0][r]), __float_as_uint(a3[1][r]), false, false);
      const unsigned v = __float_as_uint(__uint_as_float(t[0]) + dppf<XOR8>(__uint_as_float(t[1])));
      o01[r] = v == SENT ? SENT - 1u : v;
    }
    if (n < 8) __builtin_amdgcn_raw_buffer_store_b128(o01, rsrc(ws, WS_END), po + 256 * (c >> 1), 0, SC1);
    for (int r = 0; r < 4; ++r) {
      const unsigned v = __float_as_uint(a3[2][r] + hi_half(dppf<XOR8>(a3[2][r])));
      o2[r] = v == SENT ? SENT - 1u : v;
    }
    if (n < 8 && c < 2) __builtin_amdgcn_raw_buffer_store_b128(o2, rsrc(ws, WS_END), po + 512, 0, SC1);
  } else {
    const auto rw = rsrc(ws, WS_END);
    auto gate = [&](unsigned blocks) { SPIN(LDS_CAP) { if (ctl[C_BLK] >= blocks) break; __builtin_amdgcn_s_sleep(8); } };
    const bool gu = j < 18, dn = j >= 18 && j < 27;
    const int i0 = (14 - xg) & 7, iu = j >= 16 ? i0 : (j >> 1) + ((j >> 1) >= i0);
    const int mat = j & 1, u = 9 * xg + iu, hb = j - 18;
    gu32* flag = (gu32*)(ws + (gu ? WS_FG + (2 * u + mat) * 4 : WS_FP + (xg * 9 + hb) * 4));
    if (j < 27) {
      const int ldw = gu ? FF * 2 : DM * 2;
      const char* src = (gu ? (const char*)((mat ? w3 : w1) + 64 * u) : (const char*)(w2 + (size_t)576 * xg * DM + 64 * hb)) + (lane >> 3) * ldw + (lane & 7) * 16;
#pragma unroll 1
      for (int i = 0; i < 72; ++i) { glds16(src, (unsigned)(size_t)(lds + L_W) + i * 1024); src += 8 * ldw; vmwait<FFW - 1>(); }
    }
    auto echo_of = [](unsigned long long y) { return (unsigned)y & 0xffffffu; };
    auto chal_of = [](unsigned long long y) { return (unsigned)(y >> 24) & 0xffffffu; };
    auto word = [](unsigned echo, unsigned ch, unsigned hint) {
      return (unsigned long long)(echo & 0xffffffu) | ((unsigned long long)ch << 24) | ((unsigned long long)(hint & 0xffffu) << 48); };
    gu64* hi = (gu64*)(ws + WS_HI); gu64* ho = (gu64*)(ws + WS_HO);
    const unsigned long long yo = __hip_atomic_load(ho + blockIdx.x, RLX_AGENT), yi = __hip_atomic_load(hi + blockIdx.x, RLX_AGENT);
    {
      const u32x4 e = {SENT, SENT, SENT, SENT};
      for (int t = 0; t < 3; ++t)
        if (t < 2 || lane < 16) __builtin_amdgcn_raw_buffer_store_b128(e, rw, WS_P + (b * NH + h) * DM * 4 + (t * 64 + lane) * 16, 0, SC1);
      if (lane == 0 && j < 27) __hip_atomic_store(flag, 0u, RLX_AGENT);
    }
    const unsigned f0 = echo_of(yo), old = chal_of(yi);
    const unsigned chal = (f0 + (((old - f0 - 1u) & 0xffffffu) < 2u ? 3u : 1u)) & 0xffffffu;
    auto tile = [&](f32x4& r0, f32x4& r1) {
      const int q = n >> 2, p = n & 3;
      LAS char* wp = lds + L_W + (8 * c + q) * 128 + (gu ? 16 * p : 8 * p);
      const int o1 = gu ? 8 : 32, o2 = 64;
      const LAS char* ap = lds + L_ACT + (n & 7) * ACT_LD + 16 * c;
      f32x4 d[4];
      for (int T = 0; T < 4; ++T) d[T] = f32x4{0.f, 0.f, 0.f, 0.f};
#pragma unroll 2
      for (int s = 0; s < 18; ++s) {
        const u32x4 av = *(const LAS u32x4*)(ap + 64 * s);
        for (int T = 0; T < 4; ++T) {
          LAS char* a = wp + s * 4096 + (T & 1) * o1 + (T >> 1) * o2;
          const u32x2 lo = trread(a), hi2 = trread(a + 512);
          d[T] = mfma16(u32x4{lo[0], lo[1], hi2[0], hi2[1]}, av, d[T]);
        }
      }
      if (gu) { r0 = n < 8 ? d[0] : d[2]; r1 = n < 8 ? d[1] : d[3]; }
      else { r0 = n < 8 ? d[0] : d[1]; r1 = n < 8 ? d[2] : d[3]; }
    };
    u32x4 xd[9];
    if (gu) for (int t = 0; t < 9; ++t) xd[t] = *(const u32x4*)(x + (lane >> 3) * DM + 8 * ((lane & 7) + 8 * t));
    vmwait<0>();
    if (blockIdx.x != LEADER && lane == 0) __hip_atomic_store(hi + blockIdx.x, word(chal_of(yo), chal, tent), RLX_AGENT);
    if (gu) {
      float sm = 0.f, sq = 0.f;
      for (int t = 0; t < 9; ++t) for (int e = 0; e < 4; ++e) sm += bflo(xd[t][e]) + bfhi(xd[t][e]);
      sm += dppf<XOR1>(sm); sm += dppf<XOR2>(sm); sm += dppf<XOR7>(sm);
      const float mean = sm * (1.0f / DM);
      for (int t = 0; t < 9; ++t) for (int e = 0; e < 4; ++e) { const float a = bflo(xd[t][e]) - mean, d = bfhi(xd[t][e]) - mean; sq += a * a + d * d; }
      sq += dppf<XOR1>(sq); sq += dppf<XOR2>(sq); sq += dppf<XOR7>(sq);
      const LnStat st = ln_finish(mean, sq * (1.0f / DM));
      for (int t = 0; t < 9; ++t) {
        u32x4 v;
        for (int e = 0; e < 4; ++e) v[e] = pack2(ln_apply(bflo(xd[t][e]), st), ln_apply(bfhi(xd[t][e]), st));
        *(LAS u32x4*)(lds + L_ACT + (lane >> 3) * ACT_LD + 16 * ((lane & 7) + 8 * t)) = v;
      }
      f32x4 r0, r1;
      tile(r0, r1);
      const u32x4 o = {pack2(r0[0], r0[1]), pack2(r0[2], r0[3]), pack2(r1[0], r1[1]), pack2(r1[2], r1[3])};
      __builtin_amdgcn_raw_buffer_store_b128(o, rw, WS_GU + ((mat * NB + (n & 7)) * FF + 64 * u + 32 * (n >> 3) + 8 * c) * 2, 0, SC1);
      vmwait<0>();
      if (lane == 0) __hip_atomic_store(flag, 1u, RLX_AGENT);
    }
    if (blockIdx.x == LEADER) {
      unsigned long long y[4]; unsigned cl[4], sent[4];
      gate(G_L1);
      for (int q = 0; q < 4; ++q) { y[q] = __hip_atomic_load(hi + q * 64 + lane, RLX_AGENT); cl[q] = (echo_of(y[q]) + 1u) & 0xffffffu; sent[q] = ~0u; }
      SPIN(SPIN_CAP) {
        bool ok = true;
        for (int q = 0; q < 4; ++q) ok = ok && (echo_of(y[q]) == cl[q] || q * 64 + lane == LEADER);
        if (__all(ok)) break;
        for (int q = 0; q < 4; ++q)
          if (sent[q] != chal_of(y[q])) { sent[q] = chal_of(y[q]); __hip_atomic_store(ho + q * 64 + lane, word(sent[q], cl[q], 0u), RLX_AGENT); }
        gate(spins == 0 ? G_L1 + 1 : G_L2);
        __builtin_amdgcn_s_sleep(32);
        for (int q = 0; q < 4; ++q) y[q] = __hip_atomic_load(hi + q * 64 + lane, RLX_AGENT);
      }
      float ls = 0.f;
      for (int q = 0; q < 4; ++q) ls += q * 64 + lane == LEADER ? 0.f : (float)(short)((unsigned)(y[q] >> 48) - tent);
      const int mean = (int)(wave_sum(ls) * (1.0f / 255.f));
      for (int q = 0; q < 4; ++q) __hip_atomic_store(ho + q * 64 + lane, word(chal_of(y[q]) + 1u, cl[q], mean + tent), RLX_AGENT);
      ctl[C_SHIFT] = mean;
    } else {
      gate(G_HS);
      SPIN(SPIN_CAP) {
        const unsigned long long y = __hip_atomic_load(ho + blockIdx.x, RLX_AGENT);
        if (((__builtin_amdgcn_readfirstlane(echo_of(y)) - chal) & 0xffffffu) < 2u) {
          if (lane == 0) __hip_atomic_store(hi + blockIdx.x, word(chal_of(y), chal, ctl[C_LAG] + tent), RLX_AGENT);
          break;
        }
        __builtin_amdgcn_s_sleep(64);
      }
      gate(G_GO);
      SPIN(SPIN_CAP) {
        const unsigned long long y = __hip_atomic_load(ho + blockIdx.x, RLX_AGENT);
        if (__builtin_amdgcn_readfirstlane(echo_of(y)) == ((chal + 1u) & 0xffffffu)) { ctl[C_SHIFT] = (int)(short)((unsigned)(y >> 48) - tent); break; }
        __builtin_amdgcn_s_sleep(64);
      }
    }
    asm volatile("" ::: "memory");
    if (dn) {
      gu32* fg = (gu32*)(ws + WS_FG) + 18 * xg + (lane < 18 ? lane : 0);
      SPIN(SPIN_CAP) { if (__all(__hip_atomic_load(fg, RLX_AGENT) == 1u)) break; __builtin_amdgcn_s_sleep(64); }
      asm volatile("" ::: "memory");
#pragma unroll 1
      for (int t3 = 0; t3 < 3; ++t3) {
        u32x4 gv[3], uv[3]; int lo[3];
        for (int t = 0; t < 3; ++t) {
          const int q = lane + 64 * (3 * t3 + t), bb = q / 72, ch = q - 72 * bb, go = WS_GU + (bb * FF + 576 * xg + 8 * ch) * 2;
          gv[t] = __builtin_amdgcn_raw_buffer_load_b128(rw, go, 0, SC1); uv[t] = __builtin_amdgcn_raw_buffer_load_b128(rw, go + NB * FF * 2, 0, SC1);
          lo[t] = L_ACT + bb * ACT_LD + 16 * ch;
        }
        for (int t = 0; t < 3; ++t) {
          u32x4 hv;
          for (int e = 0; e < 4; ++e) hv[e] = pack2(rbf(silu(bflo(gv[t][e]))) * bflo(uv[t][e]), rbf(silu(bfhi(gv[t][e]))) * bfhi(uv[t][e]));
          *(LAS u32x4*)(lds + lo[t]) = hv;
        }
      }
      f32x4 r0, r1;
      tile(r0, r1);
      const int fo = WS_PF + ((xg * NB + (n & 7)) * DM + 64 * hb + 16 * (n >> 3) + 4 * c) * 4;
      __builtin_amdgcn_raw_buffer_store_b128(__builtin_bit_cast(u32x4, r0), rw, fo, 0, SC1);
      __builtin_amdgcn_raw_buffer_store_b128(__builtin_bit_cast(u32x4, r1), rw, fo + 128, 0, SC1);
      vmwait<0>();
      if (lane == 0) __hip_atomic_store(flag, 1u, RLX_AGENT);
    }
    const int cons = (h & 1) == 0 ? (h >> 1) : (h < 4 ? 16 + (h >> 1) : -1);
    if (cons < 0) { __syncthreads(); return; }
    const int pl = lane >> 3, col = 32 * cons + 4 * (lane & 7);
    const int ocol = b * DM + col + 2 * (c & 1) + (n >> 3);
    gate(G_F);
    float xx = bflo(x[ocol]), gg;
    {
      gu32* fp = (gu32*)(ws + WS_FP) + 9 * pl + (cons >> 1);
      SPIN(SPIN_CAP) { if (__all(__hip_atomic_load(fp, RLX_AGENT) == 1u)) break; __builtin_amdgcn_s_sleep(64); }
      asm volatile("" ::: "memory");
      gg = rbf(silu(rbf(reduce8(__builtin_bit_cast(f32x4, __builtin_amdgcn_raw_buffer_load_b128(rw, WS_PF + ((pl * NB + b) * DM + col) * 4, 0, SC1)), n & 8))));
      pin(gg); pin(xx);
    }
    __syncthreads();
    __builtin_amdgcn_s_sleep(POLL_DELAY);
    u32x4 pv[4];
    int po[4];
    for (int t = 0; t < 4; ++t) { pv[t] = u32x4{SENT, SENT, SENT, SENT}; po[t] = WS_P + ((b * NH + 8 * t + pl) * DM + col) * 4; }
    SPIN(16 * SPIN_CAP) {
      asm volatile("" ::: "memory");
      u32x4 v[4];
      for (int t = 0; t < 4; ++t) v[t] = __builtin_amdgcn_raw_buffer_load_b128(rw, po[t], 0, SC1);
      bool ok = true;
      for (int t = 0; t < 4; ++t) {
        const bool had = po[t] == WS_END, got = v[t][0] != SENT && v[t][1] != SENT && v[t][2] != SENT && v[t][3] != SENT;
        for (int r = 0; r < 4; ++r) pv[t][r] = had ? pv[t][r] : v[t][r];
        po[t] = got ? WS_END : po[t];
        ok = ok && got;
      }
      if (__all(ok)) break;
    }
    f32x4 s4;
    for (int r = 0; r < 4; ++r) s4[r] = (__uint_as_float(pv[0][r]) + __uint_as_float(pv[1][r])) + (__uint_as_float(pv[2][r]) + __uint_as_float(pv[3][r]));
    const float o = xx + rbf(rbf(reduce8(s4, n & 8)) * gg);
    if (lane < 32) out[ocol] = (u16)pack2(o, 0.f);
  }
}

extern "C" void kernel_launch(void* const* d_in, const int* in_sizes, int n_in, void* d_out, int out_size, void* d_ws, size_t ws_size, hipStream_t stream) {
  k_main<<<256, 512, 0, stream>>>((const u16*)d_in[0], (const u16*)d_in[1], (const u16*)d_in[2], (const u16*)d_in[3], (const u16*)d_in[4], (const u16*)d_in[6],
                                  (const u16*)d_in[7], (const u16*)d_in[8], (char*)d_ws, (u16*)d_out);
}
```

```cpp
#include <hip/hip_runtime.h>
#include <stdint.h>

typedef unsigned short u16;
typedef unsigned u32x4 __attribute__((ext_vector_type(4)));
typedef unsigned u32x2 __attribute__((ext_vector_type(2)));
typedef float f32x4 __attribute__((ext_vector_type(4)));
typedef short s16x4 __attribute__((ext_vector_type(4)));
typedef __bf16 bf16x8 __attribute__((ext_vector_type(8)));
typedef __bf16 bf16x2 __attribute__((ext_vector_type(2)));
#define LAS __attribute__((address_space(3)))
typedef __attribute__((address_space(1))) unsigned gu32;
typedef __attribute__((address_space(1))) unsigned long long gu64;
#define RLX_AGENT __ATOMIC_RELAXED, __HIP_MEMORY_SCOPE_AGENT

constexpr int NB = 8, DM = 576, NH = 32, SK = 4096, DH = 128, FF = 4608, HD = NH * DH;

constexpr int PKW = 4, PQW = 5, FFW = 7;
constexpr int XPRE = 11;
constexpr int WO_T0 = 1400, WO_DT = 70;
constexpr int PC_T3 = 1399, PC_RK = 229, PC_RV = 219;
constexpr int PC_L0 = 19, PC_G = 14;
constexpr int G_L1 = 5, G_HS = 7, G_L2 = 8, G_GO = 10, G_D = 11;
constexpr int NRND = 5, RND0 = 11, RNDS = 4, RND_F = 2;
constexpr int POLL_DELAY = 40;
constexpr bool ECHO_FIRST = true;
constexpr int WIN = 7;
constexpr int NPRE = 2;

constexpr int WS_P = 0, WS_PF = WS_P + NB * NH * DM * 4, WS_GU = WS_PF + 8 * NB * DM * 4, WS_FG = WS_GU + 2 * NB * FF * 2, WS_FP = WS_FG + 1024,
              WS_HI = WS_FP + 1024, WS_HO = WS_HI + 256 * 8, WS_RP = WS_HO + 256 * 8, WS_BC = WS_RP + NRND * 128,
              WS_END = WS_BC + NRND * 8 * 128;
constexpr int LEADER = 255;
constexpr unsigned SENT = 0xffffffffu;
constexpr unsigned SPIN_CAP = 1u << 18, LDS_CAP = 1u << 22;
constexpr int ACT_LD = DM * 2 + 16;
constexpr int L_W = 0;
constexpr int L_ACT = L_W + DM * 128;
constexpr int L_SC = L_ACT + NB * ACT_LD;
constexpr int L_QP = L_SC + 4 * 16 * 64 * 2;
constexpr int L_MRG = L_QP + 3 * 128 * 4;
constexpr int L_CTL = L_MRG + 4 * 132 * 4;
constexpr int L_END = L_CTL + 32;
enum { C_Q = 0, C_BLK = 1, C_ML = 2, C_LAG = 3, C_SHIFT = 4 };

template <int I> struct IC { static constexpr int v = I; };
template <int N, int I = 0, typename Fn> __device__ __forceinline__ void sfor(Fn&& f) {
  if constexpr (I < N) { f(IC<I>{}); sfor<N, I + 1>(f); }
}
#define SCHED() __builtin_amdgcn_sched_barrier(0)
#define SPIN(cap) _Pragma("unroll 1") for (unsigned spins = 0; spins < (cap); ++spins)
template <int N> __device__ __forceinline__ void vmwait() { asm volatile("s_waitcnt vmcnt(%0)" ::"n"(N) : "memory"); }
template <int I> __device__ __forceinline__ void vmwait_l(int lvl) {
  constexpr int br = ((I & 1) << 3) | ((I & 2) << 1) | ((I & 4) >> 1) | ((I & 8) >> 3);
  asm volatile("s_cmp_gt_i32 %0, %1\n\ts_cbranch_scc1 3f\n\ts_cmp_gt_i32 %0, %2\n\ts_cbranch_scc1 2f\n\ts_cmp_gt_i32 %0, %3\n\ts_cbranch_scc1 1f\n\t"
               "s_waitcnt vmcnt(3)\n1:\n\ts_waitcnt vmcnt(4)\n2:\n\ts_waitcnt vmcnt(5)\n3:\n\ts_waitcnt vmcnt(6)"
               ::"s"(lvl), "n"(32 + br), "n"(16 + br), "n"(br) : "memory", "scc");
}
__device__ __forceinline__ unsigned now() { return (unsigned)__builtin_amdgcn_s_memrealtime(); }

__device__ __forceinline__ float bflo(unsigned x) { return __uint_as_float(x << 16); }
__device__ __forceinline__ float bfhi(unsigned x) { return __uint_as_float(x & 0xffff0000u); }
__device__ __forceinline__ unsigned pack2(float lo, float hi) { bf16x2 v = {(__bf16)lo, (__bf16)hi}; return __builtin_bit_cast(unsigned, v); }
__device__ __forceinline__ float rbf(float x) { return bflo((unsigned)__builtin_bit_cast(u16, (__bf16)x)); }
template <int CTRL> __device__ __forceinline__ float dppf(float x) {
  return __builtin_bit_cast(float, __builtin_amdgcn_mov_dpp(__builtin_bit_cast(int, x), CTRL, 0xf, 0xf, true));
}
constexpr int XOR1 = 0xB1, XOR2 = 0x4E, XOR7 = 0x141, XOR8 = 0x128, UP4 = 0x104, DOWN4 = 0x114;
template <int N> __device__ __forceinline__ unsigned bcast16(unsigned x) {
  return (unsigned)__builtin_amdgcn_mov_dpp((int)x, 0x150 + N, 0xf, 0xf, true);
}
__device__ __forceinline__ float x16(float x, bool mx) {
  auto s = __builtin_amdgcn_permlane16_swap(__float_as_uint(x), __float_as_uint(x), false, false);
  x = mx ? fmaxf(__uint_as_float(s[0]), __uint_as_float(s[1])) : __uint_as_float(s[0]) + __uint_as_float(s[1]);
  auto t = __builtin_amdgcn_permlane32_swap(__float_as_uint(x), __float_as_uint(x), false, false);
  return mx ? fmaxf(__uint_as_float(t[0]), __uint_as_float(t[1])) : __uint_as_float(t[0]) + __uint_as_float(t[1]);
}
__device__ __forceinline__ float wave_sum(float v) {
  v += dppf<XOR1>(v); v += dppf<XOR2>(v); v += dppf<XOR7>(v); v += dppf<XOR8>(v); return x16(v, false);
}
__device__ __forceinline__ float wave_max(float v) {
  v = fmaxf(v, dppf<XOR1>(v)); v = fmaxf(v, dppf<XOR2>(v)); v = fmaxf(v, dppf<XOR7>(v)); v = fmaxf(v, dppf<XOR8>(v)); return x16(v, true);
}
__device__ __forceinline__ float hi_half(float x) {
  auto t = __builtin_amdgcn_permlane32_swap(__float_as_uint(x), __float_as_uint(x), false, false);
  return __uint_as_float(t[1]);
}
template <int CH, int CL> __device__ __forceinline__ float tmerge(float lo, float hi, bool b) {
  const float u = hi + dppf<CH>(hi), v = lo + dppf<CL>(lo);
  return b ? u : v;
}
__device__ __forceinline__ float reduce8(const f32x4& v, bool b3) {
  const float u = tmerge<XOR8, XOR8>(v[0], v[1], b3), t = tmerge<XOR8, XOR8>(v[2], v[3], b3);
  auto a = __builtin_amdgcn_permlane16_swap(__float_as_uint(u), __float_as_uint(t), false, false);
  const float z = __uint_as_float(a[0]) + __uint_as_float(a[1]);
  auto e = __builtin_amdgcn_permlane32_swap(__float_as_uint(z), __float_as_uint(z), false, false);
  return __uint_as_float(e[0]) + __uint_as_float(e[1]);
}
__device__ __forceinline__ void pin(float& x) { asm volatile("" : "+v"(x)); }
__device__ __forceinline__ f32x4 mfma16(u32x4 a, u32x4 b, f32x4 c) {
  return __builtin_amdgcn_mfma_f32_16x16x32_bf16(__builtin_bit_cast(bf16x8, a), __builtin_bit_cast(bf16x8, b), c, 0, 0, 0);
}
__device__ __forceinline__ float dot2(unsigned a, unsigned b, float c) {
  return __builtin_amdgcn_fdot2_f32_bf16(__builtin_bit_cast(bf16x2, a), __builtin_bit_cast(bf16x2, b), c, false);
}
__device__ __forceinline__ float silu(float x) { return x / (1.0f + __expf(-x)); }
__device__ __forceinline__ u32x4 diag(unsigned pk, const u32x4& sel) {
  u32x4 a;
  for (int i = 0; i < 4; ++i) a[i] = __builtin_amdgcn_perm(0u, pk, sel[i]);
  return a;
}
__device__ __forceinline__ auto rsrc(const void* p, int bytes) { return __builtin_amdgcn_make_buffer_rsrc((void*)p, 0, bytes, 0x00020000); }
constexpr int NT = 2, SC1 = 16;
__device__ __forceinline__ void glds16(const void* gsrc, unsigned lds_dst) {
  unsigned keep;
  asm volatile("s_mov_b32 %0, m0\n\ts_mov_b32 m0, %2\n\ts_nop 0\n\tglobal_load_lds_dwordx4 %1, off nt\n\ts_mov_b32 m0, %0" : "=&s"(keep) : "v"(gsrc), "s"(lds_dst) : "memory");
}
__device__ __forceinline__ u32x2 trread(LAS char* p) { return __builtin_bit_cast(u32x2, __builtin_amdgcn_ds_read_tr16_b64_v4i16((LAS s16x4*)p)); }

struct LnStat { float m, den, inv; };
__device__ __forceinline__ LnStat ln_finish(float mean, float var) {
  const float den = rbf(sqrtf(rbf(rbf(var) + 1e-5f)));
  return LnStat{rbf(mean), den, 1.0f / den};
}
__device__ __forceinline__ float ln_apply(float x, const LnStat& s) {
  const float a = rbf(x - s.m), q = a * s.inv;
  return fmaf(fmaf(-q, s.den, a), s.inv, q);
}

__global__ void __launch_bounds__(512, 2) k_main(const u16* __restrict__ x, const u16* __restrict__ K, const u16* __restrict__ V, const u16* __restrict__ wq,
                                                 const u16* __restrict__ wo, const u16* __restrict__ w1, const u16* __restrict__ w3,
                                                 const u16* __restrict__ w2, char* ws, u16* __restrict__ out) {
  __shared__ __attribute__((aligned(16))) char lds_[L_END];
  LAS char* lds = (LAS char*)lds_;
  asm volatile("" ::"s"(x), "s"(K), "s"(V), "s"(wq), "s"(w1), "s"(w3), "s"(w2), "s"(ws));
  const int tid = threadIdx.x, lane = tid & 63, w = __builtin_amdgcn_readfirstlane(tid >> 6);
  const int xg = blockIdx.x & 7, j0 = blockIdx.x >> 3, b = j0 >> 2;
  const int h = (j0 & 1) | (xg << 1) | ((j0 & 2) << 3), j = (j0 & 1) * 16 + (j0 >> 1);
  const unsigned tent = now();
  const int c = lane >> 4, n = lane & 15;
  volatile LAS unsigned* ctl = (volatile LAS unsigned*)(lds + L_CTL);
  if (tid < 8) ctl[tid] = 0u;
  __syncthreads();
  u32x4 sel;
  {
    const unsigned src = (n < 8) ? 0x0100u : 0x0302u; const int jj = n & 7;
    for (int i = 0; i < 4; ++i) sel[i] = ((jj >> 1) != i) ? 0x0c0c0c0cu : ((jj & 1) ? (0x0c0cu | (src << 16)) : (0x0c0c0000u | src));
  }
  if (w < 4) {
    const size_t base = ((size_t)(b * NH + h) * SK) * DH;
    const char* Kb = (const char*)(K + base); const char* Vb = (const char*)(V + base);
    const int voff = lane * 16 + w * 4096;
    u32x4 ring[16], pre[16 * NPRE];
    auto ld = [&](const char* blk, auto ic) { constexpr int i = ic.v;
      ring[i] = __builtin_amdgcn_raw_buffer_load_b128(rsrc(blk, 65536), voff + (i & 3) * 1024, (i >> 2) * 16384, NT); };
    sfor<PKW>([&](auto ic) { ld(Kb, ic); });
    sfor<16 - PKW>([&](auto ic) { SCHED(); vmwait<PKW - 1>(); SCHED(); ld(Kb, IC<ic.v + PKW>{}); });
    sfor<16 * NPRE>([&](auto ic) { constexpr int i = ic.v; SCHED(); vmwait<PKW - 1>(); SCHED();
      pre[i] = __builtin_amdgcn_raw_buffer_load_b128(rsrc(Kb + 65536, NPRE * 65536), voff + (i & 3) * 1024, (i >> 2) * 16384, NT); });
    SCHED();
    __builtin_amdgcn_s_setprio(3);
    SPIN(LDS_CAP) { if (ctl[C_Q] >= 3u) break; __builtin_amdgcn_s_sleep(1); }
    asm volatile("" ::: "memory");
    unsigned qd[4];
    {
      f32x4 a = {0.f, 0.f, 0.f, 0.f}, e = a;
      for (int i = 0; i < 3; ++i) { a += *(LAS f32x4*)(lds + L_QP + i * 512 + 32 * n); e += *(LAS f32x4*)(lds + L_QP + i * 512 + 32 * n + 16); }
      qd[0] = pack2(a[0], a[1]); qd[1] = pack2(a[2], a[3]); qd[2] = pack2(e[0], e[1]); qd[3] = pack2(e[2], e[3]);
    }
    LAS u16* sc = (LAS u16*)(lds + L_SC) + w * 1024 + lane;
    float mx = -INFINITY;
    int lvl = PC_L0;
    unsigned due = tent + (PC_T3 - PC_RK);
    auto pace = [&](int ticks) {
      due += ticks;
      const int shift = __builtin_amdgcn_readfirstlane(ctl[C_SHIFT]);
      const int lag = min(max((int)(now() - due), -4096), 4096);
      if (w == 0) ctl[C_LAG] = lag;
      lvl = min(max(PC_L0 + ((lag - shift) * PC_G >> 6), 0), 48);
    };
    const bool b0 = n & 1, b1 = n & 2, b2 = n & 4, b3 = n & 8;
    auto kblock = [&](const char* cur, const char* nxt, int blk, auto mode) {
      float t0, t1, t2, t3;
      sfor<16>([&](auto ic) { constexpr int i = ic.v;
        const u32x4 kv = mode.v >= 2 ? pre[(mode.v >= 2 ? mode.v - 2 : 0) * 16 + i] : ring[i];
        if constexpr (mode.v == 0 || (mode.v == -1 && i + WIN >= XPRE)) {
          SCHED(); vmwait_l<i>(lvl); SCHED(); if constexpr (i + WIN >= 16) ld(nxt, IC<i + WIN - 16>{}); else ld(cur, IC<i + WIN>{}); SCHED(); }
        float d = dot2(kv[0], qd[0], 0.f); d = dot2(kv[1], qd[1], d); d = dot2(kv[2], qd[2], d); d = dot2(kv[3], qd[3], d);
        pin(d);
        SCHED();
        if constexpr (mode.v == 1 && i < XPRE) { ld(nxt, ic); SCHED(); }
        if constexpr (!(i & 1)) t0 = d;
        else {
          d = tmerge<XOR1, XOR1>(t0, d, b0);
          if constexpr (!(i & 2)) t1 = d;
          else {
            d = tmerge<XOR2, XOR2>(t1, d, b1);
            if constexpr (!(i & 4)) t2 = d;
            else {
              d = tmerge<DOWN4, UP4>(t2, d, b2);
              if constexpr (!(i & 8)) t3 = d;
              else {
                d = tmerge<XOR8, XOR8>(t3, d, b3);
                d = rbf(rbf(d) * 0.08838834764831845f);
                mx = fmaxf(mx, d);
                sc[blk * 64] = (u16)(__float_as_uint(d) >> 16);
                if (w == 0) ctl[C_BLK] = blk + 1;
              }
            }
          }
        }
      });
    };
    constexpr int B0 = NPRE + 1;
    kblock(Kb, Kb + B0 * 65536, 0, IC<1>{});
    sfor<NPRE>([&](auto pc) { kblock(Kb, Kb, 1 + pc.v, IC<2 + pc.v>{}); });
    kblock(Kb + B0 * 65536, Kb + (B0 + 1) * 65536, B0, IC<-1>{});
    pace(PC_RK);
#pragma unroll 1
    for (int blk = B0 + 1; blk < 16; ++blk) { kblock(Kb + blk * 65536, blk < 15 ? Kb + (blk + 1) * 65536 : Vb, blk, IC<0>{}); pace(PC_RK); }
    mx = wave_max(mx);
    float l = 0.f;
    f32x4 acc = {0.f, 0.f, 0.f, 0.f};
    float s = bflo(sc[0]);
    LAS float* mr = (LAS float*)(lds + L_MRG) + w * 132;
    auto vblock = [&](const char* cur, const char* nxt, int blk, auto last) {
      if (w == 0) ctl[C_BLK] = 16 + blk;
      const float p = __expf(s - mx);
      l += p;
      const float ph = rbf(p);
      const unsigned pk = pack2(ph, p - ph);
      if constexpr (!last.v) s = bflo(sc[(blk + 1) * 64]);
      else {
        l = wave_sum(l);
        if (lane == 0) { mr[0] = mx; mr[1] = l; }
        asm volatile("s_waitcnt lgkmcnt(0)" ::: "memory");
        if (lane == 0) __hip_atomic_fetch_add((LAS unsigned*)(lds + L_CTL) + C_ML, 1u, __ATOMIC_RELAXED, __HIP_MEMORY_SCOPE_WORKGROUP);
      }
      sfor<16>([&](auto ic) { constexpr int i = ic.v;
        const u32x4 a = diag(bcast16<i>(pk), sel), vv = ring[i];
        SCHED();
        if constexpr (!last.v || i + WIN < 16) vmwait_l<i>(lvl);
        SCHED();
        if constexpr (i + WIN < 16) ld(cur, IC<i + WIN>{}); else if constexpr (!last.v) ld(nxt, IC<i + WIN - 16>{});
        SCHED();
        acc = mfma16(a, vv, acc);
        SCHED();
      });
    };
#pragma unroll 1
    for (int blk = 0; blk < 15; ++blk) { vblock(Vb + blk * 65536, Vb + (blk + 1) * 65536, blk, IC<0>{}); pace(PC_RV); }
    vblock(Vb + 15 * 65536, Vb, 15, IC<1>{});
    for (int r = 0; r < 2; ++r) {
      auto t = __builtin_amdgcn_permlane32_swap(__float_as_uint(acc[r]), __float_as_uint(acc[r + 2]), false, false);
      mr[4 + (4 * (c & 1) + 2 * (c >> 1) + r) * 16 + n] = __uint_as_float(t[0]) + __uint_as_float(t[1]);
    }
    __syncthreads();
  } else if (w < 7) {
    const int i4 = w - 4;
    {
      const unsigned* xr = (const unsigned*)(x + b * DM);
      unsigned d[5];
      for (int t = 0; t < 4; ++t) d[t] = xr[lane + 64 * t];
      d[4] = xr[256 + (lane & 31)];
      const u16* xe = x + b * DM + 192 * i4 + c;
      const unsigned e0 = xe[8 * n], e1 = xe[8 * n + 4], e2 = xe[128 + 8 * (n & 7)], e3 = xe[128 + 8 * (n & 7) + 4];
      SCHED();
      u32x4 wr[48];
      const auto rq = rsrc(wq, DM * HD * 2);
      const int vo = (4 * (n >> 3) + c) * (HD * 2) + (n & 7) * 16;
      const int so = 192 * i4 * (HD * 2) + h * 256;
      auto ldq = [&](auto ic) { constexpr int i = ic.v;
        if constexpr (i < 48) wr[i] = __builtin_amdgcn_raw_buffer_load_b128(rq, vo + (i & 1) * 128, so + (i >> 1) * 8 * (HD * 2), NT); };
      sfor<PQW>(ldq);
      SCHED();
      float sm = 0.f, sq = 0.f;
      for (int t = 0; t < 5; ++t) sm += (t < 4 || lane < 32) ? bflo(d[t]) + bfhi(d[t]) : 0.f;
      const float mean = wave_sum(sm) * (1.0f / DM);
      for (int t = 0; t < 5; ++t) { const float a = bflo(d[t]) - mean, e = bfhi(d[t]) - mean; sq += (t < 4 || lane < 32) ? a * a + e * e : 0.f; }
      const LnStat st = ln_finish(mean, wave_sum(sq) * (1.0f / DM));
      const unsigned pk0 = pack2(ln_apply(bflo(e0), st), ln_apply(bflo(e1), st)), pk1 = pack2(ln_apply(bflo(e2), st), ln_apply(bflo(e3), st));
      f32x4 a0 = {0.f, 0.f, 0.f, 0.f}, a1 = a0;
      sfor<24>([&](auto sc_) { constexpr int s = sc_.v;
        const u32x4 a = diag(bcast16<s & 15>(s < 16 ? pk0 : pk1), sel);
        a0 = mfma16(a, wr[2 * s], a0); SCHED(); ldq(IC<2 * s + PQW>{}); SCHED();
        a1 = mfma16(a, wr[2 * s + 1], a1); SCHED(); ldq(IC<2 * s + 1 + PQW>{}); SCHED(); });
      f32x4 o0, o1;
      for (int r = 0; r < 4; ++r) { o0[r] = a0[r] + hi_half(dppf<XOR8>(a0[r])); o1[r] = a1[r] + hi_half(dppf<XOR8>(a1[r])); }
      if (c < 2 && n < 8) { *(LAS f32x4*)(lds + L_QP + i4 * 512 + 32 * n + 16 * c) = o0; *(LAS f32x4*)(lds + L_QP + i4 * 512 + 256 + 32 * n + 16 * c) = o1; }
      asm volatile("s_waitcnt lgkmcnt(0)" ::: "memory");
      if (lane == 0) __hip_atomic_fetch_add((LAS unsigned*)(lds + L_CTL) + C_Q, 1u, __ATOMIC_RELAXED, __HIP_MEMORY_SCOPE_WORKGROUP);
    }
    u32x4 wr[48];
    const auto ro = rsrc(wo, HD * DM * 2);
    const int vo = (4 * (n >> 3) + c) * (DM * 2) + (n & 7) * 16;
    const int so = 128 * h * (DM * 2) + 384 * i4;
    sfor<48>([&](auto ic) { constexpr int i = ic.v;
      SPIN(LDS_CAP) { if (now() - tent >= (unsigned)(WO_T0 + i * WO_DT)) break; __builtin_amdgcn_s_sleep(2); }
      wr[i] = __builtin_amdgcn_raw_buffer_load_b128(ro, vo + (i % 3) * 128, so + (i / 3) * 8 * (DM * 2), NT);
      SCHED(); vmwait<1>(); SCHED(); });
    const LAS float* mr = (const LAS float*)(lds + L_MRG);
    SPIN(LDS_CAP) { if (ctl[C_ML] >= 4u) break; __builtin_amdgcn_s_sleep(4); }
    asm volatile("" ::: "memory");
    float f4[4], M = -INFINITY, L = 0.f;
    for (int i = 0; i < 4; ++i) { f4[i] = mr[i * 132]; M = fmaxf(M, f4[i]); }
    for (int i = 0; i < 4; ++i) { f4[i] = __expf(f4[i] - M); L += f4[i] * mr[i * 132 + 1]; }
    float linv = 1.0f / L;
    for (int i = 0; i < 4; ++i) pin(f4[i]);
    pin(linv);
    __syncthreads();
    float oa = 0.f, ob = 0.f;
    for (int i = 0; i < 4; ++i) { oa += f4[i] * mr[i * 132 + 4 + c * 16 + n]; ob += f4[i] * mr[i * 132 + 4 + (4 + c) * 16 + n]; }
    const unsigned pk = pack2(oa * linv, ob * linv);
    f32x4 a3[3] = {{0.f, 0.f, 0.f, 0.f}, {0.f, 0.f, 0.f, 0.f}, {0.f, 0.f, 0.f, 0.f}};
    sfor<16>([&](auto sc_) { constexpr int s = sc_.v;
      const u32x4 a = diag(bcast16<s>(pk), sel);
      for (int t = 0; t < 3; ++t) a3[t] = mfma16(a, wr[3 * s + t], a3[t]); });
    const int po = WS_P + ((b * NH + h) * DM + 192 * i4 + 8 * n + 4 * (c & 1)) * 4;
    u32x4 o01, o2;
    for (int r = 0; r < 4; ++r) {
      auto t = __builtin_amdgcn_permlane32_swap(__float_as_uint(a3[0][r]), __float_as_uint(a3[1][r]), false, false);
      const unsigned v = __float_as_uint(__uint_as_float(t[0]) + dppf<XOR8>(__uint_as_float(t[1])));
      o01[r] = v == SENT ? SENT - 1u : v;
    }
    if (n < 8) __builtin_amdgcn_raw_buffer_store_b128(o01, rsrc(ws, WS_END), po + 256 * (c >> 1), 0, SC1);
    for (int r = 0; r < 4; ++r) {
      const unsigned v = __float_as_uint(a3[2][r] + hi_half(dppf<XOR8>(a3[2][r])));
      o2[r] = v == SENT ? SENT - 1u : v;
    }
    if (n < 8 && c < 2) __builtin_amdgcn_raw_buffer_store_b128(o2, rsrc(ws, WS_END), po + 512, 0, SC1);
  } else {
    const auto rw = rsrc(ws, WS_END);
    auto gate = [&](unsigned blocks) { SPIN(LDS_CAP) { if (ctl[C_BLK] >= blocks) break; __builtin_amdgcn_s_sleep(8); } };
    const bool gu = j < 18, dn = j >= 18 && j < 27;
    const int i0 = (14 - xg) & 7, iu = j >= 16 ? i0 : (j >> 1) + ((j >> 1) >= i0);
    const int mat = j & 1, u = 9 * xg + iu, hb = j - 18;
    gu32* flag = (gu32*)(ws + (gu ? WS_FG + (2 * u + mat) * 4 : WS_FP + (xg * 9 + hb) * 4));
    if (j < 27) {
      const int ldw = gu ? FF * 2 : DM * 2;
      const char* src = (gu ? (const char*)((mat ? w3 : w1) + 64 * u) : (const char*)(w2 + (size_t)576 * xg * DM + 64 * hb)) + (lane >> 3) * ldw + (lane & 7) * 16;
#pragma unroll 1
      for (int i = 0; i < 72; ++i) { glds16(src, (unsigned)(size_t)(lds + L_W) + i * 1024); src += 8 * ldw; vmwait<FFW - 1>(); }
    }
    auto echo_of = [](unsigned long long y) { return (unsigned)y & 0xffffffu; };
    auto chal_of = [](unsigned long long y) { return (unsigned)(y >> 24) & 0xffffffu; };
    auto word = [](unsigned echo, unsigned ch, unsigned hint) {
      return (unsigned long long)(echo & 0xffffffu) | ((unsigned long long)ch << 24) | ((unsigned long long)(hint & 0xffffu) << 48); };
    const int ri = j0 == 2 ? 0 : j0 == 12 ? 1 : j0 == 23 ? 2 : j0 == 29 ? 3 : -1;
    gu64* hi = (gu64*)(ws + WS_HI); gu64* ho = (gu64*)(ws + WS_HO);
    const unsigned long long yo = __hip_atomic_load(ho + blockIdx.x, RLX_AGENT), yi = __hip_atomic_load(hi + blockIdx.x, RLX_AGENT);
    {
      const u32x4 e = {SENT, SENT, SENT, SENT};
      for (int t = 0; t < 3; ++t)
        if (t < 2 || lane < 16) __builtin_amdgcn_raw_buffer_store_b128(e, rw, WS_P + (b * NH + h) * DM * 4 + (t * 64 + lane) * 16, 0, SC1);
      if (lane == 0 && j < 27) __hip_atomic_store(flag, 0u, RLX_AGENT);
      if (ri >= 0 && lane < NRND) __hip_atomic_store((gu32*)(ws + WS_RP + lane * 128 + (xg * 4 + ri) * 4), 0u, RLX_AGENT);
      if (blockIdx.x == LEADER && lane < NRND * 8) __hip_atomic_store((gu32*)(ws + WS_BC + lane * 128), 0u, RLX_AGENT);
    }
    const unsigned f0 = echo_of(yo), old = chal_of(yi);
    const unsigned chal = (f0 + (((old - f0 - 1u) & 0xffffffu) < 2u ? 3u : 1u)) & 0xffffffu;
    auto tile = [&](f32x4& r0, f32x4& r1) {
      const int q = n >> 2, p = n & 3;
      LAS char* wp = lds + L_W + (8 * c + q) * 128 + (gu ? 16 * p : 8 * p);
      const int o1 = gu ? 8 : 32, o2 = 64;
      const LAS char* ap = lds + L_ACT + (n & 7) * ACT_LD + 16 * c;
      f32x4 d[4];
      for (int T = 0; T < 4; ++T) d[T] = f32x4{0.f, 0.f, 0.f, 0.f};
#pragma unroll 2
      for (int s = 0; s < 18; ++s) {
        const u32x4 av = *(const LAS u32x4*)(ap + 64 * s);
        for (int T = 0; T < 4; ++T) {
          LAS char* a = wp + s * 4096 + (T & 1) * o1 + (T >> 1) * o2;
          const u32x2 lo = trread(a), hi2 = trread(a + 512);
          d[T] = mfma16(u32x4{lo[0], lo[1], hi2[0], hi2[1]}, av, d[T]);
        }
      }
      if (gu) { r0 = n < 8 ? d[0] : d[2]; r1 = n < 8 ? d[1] : d[3]; }
      else { r0 = n < 8 ? d[0] : d[1]; r1 = n < 8 ? d[2] : d[3]; }
    };
    u32x4 xd[9];
    if (gu) for (int t = 0; t < 9; ++t) xd[t] = *(const u32x4*)(x + (lane >> 3) * DM + 8 * ((lane & 7) + 8 * t));
    vmwait<0>();
    if (blockIdx.x != LEADER && lane == 0) __hip_atomic_store(hi + blockIdx.x, word(chal_of(yo), chal, tent), RLX_AGENT);
    if (ECHO_FIRST && blockIdx.x != LEADER) {
      gate(G_HS);
      SPIN(SPIN_CAP) {
        const unsigned long long y = __hip_atomic_load(ho + blockIdx.x, RLX_AGENT);
        if (((__builtin_amdgcn_readfirstlane(echo_of(y)) - chal) & 0xffffffu) < 2u) {
          if (lane == 0) __hip_atomic_store(hi + blockIdx.x, word(chal_of(y), chal, ctl[C_LAG] + tent), RLX_AGENT);
          break;
        }
        __builtin_amdgcn_s_sleep(64);
      }
    }
    if (gu) {
      float sm = 0.f, sq = 0.f;
      for (int t = 0; t < 9; ++t) for (int e = 0; e < 4; ++e) sm += bflo(xd[t][e]) + bfhi(xd[t][e]);
      sm += dppf<XOR1>(sm); sm += dppf<XOR2>(sm); sm += dppf<XOR7>(sm);
      const float mean = sm * (1.0f / DM);
      for (int t = 0; t < 9; ++t) for (int e = 0; e < 4; ++e) { const float a = bflo(xd[t][e]) - mean, d = bfhi(xd[t][e]) - mean; sq += a * a + d * d; }
      sq += dppf<XOR1>(sq); sq += dppf<XOR2>(sq); sq += dppf<XOR7>(sq);
      const LnStat st = ln_finish(mean, sq * (1.0f / DM));
      for (int t = 0; t < 9; ++t) {
        u32x4 v;
        for (int e = 0; e < 4; ++e) v[e] = pack2(ln_apply(bflo(xd[t][e]), st), ln_apply(bfhi(xd[t][e]), st));
        *(LAS u32x4*)(lds + L_ACT + (lane >> 3) * ACT_LD + 16 * ((lane & 7) + 8 * t)) = v;
      }
      f32x4 r0, r1;
      tile(r0, r1);
      const u32x4 o = {pack2(r0[0], r0[1]), pack2(r0[2], r0[3]), pack2(r1[0], r1[1]), pack2(r1[2], r1[3])};
      __builtin_amdgcn_raw_buffer_store_b128(o, rw, WS_GU + ((mat * NB + (n & 7)) * FF + 64 * u + 32 * (n >> 3) + 8 * c) * 2, 0, SC1);
      vmwait<0>();
      if (lane == 0) __hip_atomic_store(flag, 1u, RLX_AGENT);
    }
    if (blockIdx.x == LEADER) {
      unsigned long long y[4]; unsigned cl[4], sent[4];
      gate(G_L1);
      for (int q = 0; q < 4; ++q) { y[q] = __hip_atomic_load(hi + q * 64 + lane, RLX_AGENT); cl[q] = (echo_of(y[q]) + 1u) & 0xffffffu; sent[q] = ~0u; }
      SPIN(SPIN_CAP) {
        bool ok = true;
        for (int q = 0; q < 4; ++q) ok = ok && (echo_of(y[q]) == cl[q] || q * 64 + lane == LEADER);
        if (__all(ok)) break;
        for (int q = 0; q < 4; ++q)
          if (sent[q] != chal_of(y[q])) { sent[q] = chal_of(y[q]); __hip_atomic_store(ho + q * 64 + lane, word(sent[q], cl[q], 0u), RLX_AGENT); }
        gate(spins == 0 ? G_L1 + 1 : G_L2);
        __builtin_amdgcn_s_sleep(32);
        for (int q = 0; q < 4; ++q) y[q] = __hip_atomic_load(hi + q * 64 + lane, RLX_AGENT);
      }
      float ls = 0.f;
      for (int q = 0; q < 4; ++q) ls += q * 64 + lane == LEADER ? 0.f : (float)(short)((unsigned)(y[q] >> 48) - tent);
      const int mean = (int)(wave_sum(ls) * (1.0f / 255.f));
      for (int q = 0; q < 4; ++q) __hip_atomic_store(ho + q * 64 + lane, word(chal_of(y[q]) + 1u, cl[q], mean + tent), RLX_AGENT);
      ctl[C_SHIFT] = mean;
    } else {
      if (!ECHO_FIRST) {
      gate(G_HS);
      SPIN(SPIN_CAP) {
        const unsigned long long y = __hip_atomic_load(ho + blockIdx.x, RLX_AGENT);
        if (((__builtin_amdgcn_readfirstlane(echo_of(y)) - chal) & 0xffffffu) < 2u) {
          if (lane == 0) __hip_atomic_store(hi + blockIdx.x, word(chal_of(y), chal, ctl[C_LAG] + tent), RLX_AGENT);
          break;
        }
        __builtin_amdgcn_s_sleep(64);
      }
      }
      gate(G_GO);
      SPIN(SPIN_CAP) {
        const unsigned long long y = __hip_atomic_load(ho + blockIdx.x, RLX_AGENT);
        if (__builtin_amdgcn_readfirstlane(echo_of(y)) == ((chal + 1u) & 0xffffffu)) { ctl[C_SHIFT] = (int)(short)((unsigned)(y >> 48) - tent); break; }
        __builtin_amdgcn_s_sleep(64);
      }
    }
    asm volatile("" ::: "memory");
    if (dn) {
      gate(G_D);
      gu32* fg = (gu32*)(ws + WS_FG) + 18 * xg + (lane < 18 ? lane : 0);
      SPIN(SPIN_CAP) { if (__all(__hip_atomic_load(fg, RLX_AGENT) == 1u)) break; __builtin_amdgcn_s_sleep(64); }
      asm volatile("" ::: "memory");
#pragma unroll 1
      for (int t3 = 0; t3 < 3; ++t3) {
        u32x4 gv[3], uv[3]; int lo[3];
        for (int t = 0; t < 3; ++t) {
          const int q = lane + 64 * (3 * t3 + t), bb = q / 72, ch = q - 72 * bb, go = WS_GU + (bb * FF + 576 * xg + 8 * ch) * 2;
          gv[t] = __builtin_amdgcn_raw_buffer_load_b128(rw, go, 0, SC1); uv[t] = __builtin_amdgcn_raw_buffer_load_b128(rw, go + NB * FF * 2, 0, SC1);
          lo[t] = L_ACT + bb * ACT_LD + 16 * ch;
        }
        for (int t = 0; t < 3; ++t) {
          u32x4 hv;
          for (int e = 0; e < 4; ++e) hv[e] = pack2(rbf(silu(bflo(gv[t][e]))) * bflo(uv[t][e]), rbf(silu(bfhi(gv[t][e]))) * bfhi(uv[t][e]));
          *(LAS u32x4*)(lds + lo[t]) = hv;
        }
      }
      f32x4 r0, r1;
      tile(r0, r1);
      const int fo = WS_PF + ((xg * NB + (n & 7)) * DM + 64 * hb + 16 * (n >> 3) + 4 * c) * 4;
      __builtin_amdgcn_raw_buffer_store_b128(__builtin_bit_cast(u32x4, r0), rw, fo, 0, SC1);
      __builtin_amdgcn_raw_buffer_store_b128(__builtin_bit_cast(u32x4, r1), rw, fo + 128, 0, SC1);
      vmwait<0>();
      if (lane == 0) __hip_atomic_store(flag, 1u, RLX_AGENT);
    }
    const int cons = (h & 1) == 0 ? (h >> 1) : (h < 4 ? 16 + (h >> 1) : -1);
    const int pl = lane >> 3, col = 32 * cons + 4 * (lane & 7);
    const int ocol = b * DM + col + 2 * (c & 1) + (n >> 3);
    float xx = 0.f, gg = 0.f;
#pragma unroll 1
    for (int r = 0; r < NRND; ++r) {
      const unsigned blk = RND0 + RNDS * r;
      gate(blk);
      if (ri >= 0 && lane == 0) __hip_atomic_store((gu32*)(ws + WS_RP + r * 128 + (xg * 4 + ri) * 4), ((ctl[C_LAG] + tent) & 0xffffu) | 0x10000u, RLX_AGENT);
      if (blockIdx.x == LEADER) {
        gate(blk + 1);
        const unsigned v = __hip_atomic_load((gu32*)(ws + WS_RP + r * 128 + (lane & 31) * 4), RLX_AGENT);
        const float cnt = wave_sum(v != 0u ? 1.f : 0.f), sum = wave_sum(v != 0u ? (float)(short)(v - tent) : 0.f);
        if (cnt >= 32.f && lane < 8) __hip_atomic_store((gu32*)(ws + WS_BC + (r * 8 + lane) * 128), (((int)(sum / cnt) + tent) & 0xffffu) | 0x10000u, RLX_AGENT);
      }
      if (r == RND_F && cons >= 0) {
        xx = bflo(x[ocol]);
        gu32* fp = (gu32*)(ws + WS_FP) + 9 * pl + (cons >> 1);
        SPIN(SPIN_CAP) { if (__all(__hip_atomic_load(fp, RLX_AGENT) == 1u)) break; __builtin_amdgcn_s_sleep(64); }
        asm volatile("" ::: "memory");
        gg = rbf(silu(rbf(reduce8(__builtin_bit_cast(f32x4, __builtin_amdgcn_raw_buffer_load_b128(rw, WS_PF + ((pl * NB + b) * DM + col) * 4, 0, SC1)), n & 8))));
      }
      gate(blk + 2);
      const unsigned m = __hip_atomic_load((gu32*)(ws + WS_BC + (r * 8 + xg) * 128), RLX_AGENT);
      if (m != 0u) ctl[C_SHIFT] = (int)(short)(m - tent);
    }
    if (cons < 0) { __syncthreads(); return; }
    __syncthreads();
    __builtin_amdgcn_s_sleep(POLL_DELAY);
    u32x4 pv[4];
    int po[4];
    for (int t = 0; t < 4; ++t) { pv[t] = u32x4{SENT, SENT, SENT, SENT}; po[t] = WS_P + ((b * NH + 8 * t + pl) * DM + col) * 4; }
    SPIN(16 * SPIN_CAP) {
      asm volatile("" ::: "memory");
      u32x4 v[4];
      for (int t = 0; t < 4; ++t) v[t] = __builtin_amdgcn_raw_buffer_load_b128(rw, po[t], 0, SC1);
      bool ok = true;
      for (int t = 0; t < 4; ++t) {
        const bool had = po[t] == WS_END, got = v[t][0] != SENT && v[t][1] != SENT && v[t][2] != SENT && v[t][3] != SENT;
        for (int r = 0; r < 4; ++r) pv[t][r] = had ? pv[t][r] : v[t][r];
        po[t] = got ? WS_END : po[t];
        ok = ok && got;
      }
      if (__all(ok)) break;
    }
    f32x4 s4;
    for (int r = 0; r < 4; ++r) s4[r] = (__uint_as_float(pv[0][r]) + __uint_as_float(pv[1][r])) + (__uint_as_float(pv[2][r]) + __uint_as_float(pv[3][r]));
    const float o = xx + rbf(rbf(reduce8(s4, n & 8)) * gg);
    if (lane < 32) out[ocol] = (u16)pack2(o, 0.f);
  }
}

extern "C" void kernel_launch(void* const* d_in, const int* in_sizes, int n_in, void* d_out, int out_size, void* d_ws, size_t ws_size, hipStream_t stream) {
  k_main<<<256, 512, 0, stream>>>((const u16*)d_in[0], (const u16*)d_in[1], (const u16*)d_in[2], (const u16*)d_in[3], (const u16*)d_in[4], (const u16*)d_in[6],
                                  (const u16*)d_in[7], (const u16*)d_in[8], (char*)d_ws, (u16*)d_out);
}
```

```cpp
#include <hip/hip_runtime.h>
#include <stdint.h>

typedef unsigned short u16;
typedef unsigned u32x4 __attribute__((ext_vector_type(4)));
typedef unsigned u32x2 __attribute__((ext_vector_type(2)));
typedef float f32x4 __attribute__((ext_vector_type(4)));
typedef short s16x4 __attribute__((ext_vector_type(4)));
typedef __bf16 bf16x8 __attribute__((ext_vector_type(8)));
typedef __bf16 bf16x2 __attribute__((ext_vector_type(2)));
#define LAS __attribute__((address_space(3)))
typedef __attribute__((address_space(1))) unsigned gu32;
typedef __attribute__((address_space(1))) unsigned long long gu64;
#define RLX_AGENT __ATOMIC_RELAXED, __HIP_MEMORY_SCOPE_AGENT

constexpr int NB = 8, DM = 576, NH = 32, SK = 4096, DH = 128, FF = 4608, HD = NH * DH;

constexpr int PK0 = 6, PKW = 4, PQW = 5, FFW = 7;
constexpr int XPRE = 11;
constexpr int WO_T0 = 1400, WO_DT = 70;
constexpr int PC_T3 = 1385, PC_RK = 229, PC_RV = 219;
constexpr int PC_L0 = 19, PC_G = 12;
constexpr int G_L1 = 5, G_HS = 7, G_L2 = 8, G_GO = 10, G_D = 11;
constexpr int NRND = 5, RND0 = 11, RNDS = 4, RND_F = 2;
constexpr int POLL_DELAY = 40;
constexpr bool ECHO_FIRST = false;
constexpr int WIN = 7;
constexpr int NPRE = 2;

constexpr int WS_P = 0, WS_PF = WS_P + NB * NH * DM * 4, WS_GU = WS_PF + 8 * NB * DM * 4, WS_FG = WS_GU + 2 * NB * FF * 2, WS_FP = WS_FG + 1024,
              WS_HI = WS_FP + 1024, WS_HO = WS_HI + 256 * 8, WS_RP = WS_HO + 256 * 8, WS_BC = WS_RP + NRND * 128,
              WS_END = WS_BC + NRND * 8 * 128;
constexpr int LEADER = 255;
constexpr unsigned SENT = 0xffffffffu;
constexpr unsigned SPIN_CAP = 1u << 18, LDS_CAP = 1u << 22;
constexpr int ACT_LD = DM * 2 + 16;
constexpr int L_W = 0;
constexpr int L_ACT = L_W + DM * 128;
constexpr int L_SC = L_ACT + NB * ACT_LD;
constexpr int L_QP = L_SC + 4 * 16 * 64 * 2;
constexpr int L_MRG = L_QP + 3 * 128 * 4;
constexpr int L_CTL = L_MRG + 4 * 132 * 4;
constexpr int L_END = L_CTL + 32;
enum { C_Q = 0, C_BLK = 1, C_ML = 2, C_LAG = 3, C_SHIFT = 4 };

template <int I> struct IC { static constexpr int v = I; };
template <int N, int I = 0, typename Fn> __device__ __forceinline__ void sfor(Fn&& f) {
  if constexpr (I < N) { f(IC<I>{}); sfor<N, I + 1>(f); }
}
#define SCHED() __builtin_amdgcn_sched_barrier(0)
#define SPIN(cap) _Pragma("unroll 1") for (unsigned spins = 0; spins < (cap); ++spins)
template <int N> __device__ __forceinline__ void vmwait() { asm volatile("s_waitcnt vmcnt(%0)" ::"n"(N) : "memory"); }
template <int I> __device__ __forceinline__ void vmwait_l(int lvl) {
  constexpr int br = ((I & 1) << 3) | ((I & 2) << 1) | ((I & 4) >> 1) | ((I & 8) >> 3);
  asm volatile("s_cmp_gt_i32 %0, %1\n\ts_cbranch_scc1 3f\n\ts_cmp_gt_i32 %0, %2\n\ts_cbranch_scc1 2f\n\ts_cmp_gt_i32 %0, %3\n\ts_cbranch_scc1 1f\n\t"
               "s_waitcnt vmcnt(3)\n1:\n\ts_waitcnt vmcnt(4)\n2:\n\ts_waitcnt vmcnt(5)\n3:\n\ts_waitcnt vmcnt(6)"
               ::"s"(lvl), "n"(32 + br), "n"(16 + br), "n"(br) : "memory", "scc");
}
__device__ __forceinline__ unsigned now() { return (unsigned)__builtin_amdgcn_s_memrealtime(); }

__device__ __forceinline__ float bflo(unsigned x) { return __uint_as_float(x << 16); }
__device__ __forceinline__ float bfhi(unsigned x) { return __uint_as_float(x & 0xffff0000u); }
__device__ __forceinline__ unsigned pack2(float lo, float hi) { bf16x2 v = {(__bf16)lo, (__bf16)hi}; return __builtin_bit_cast(unsigned, v); }
__device__ __forceinline__ float rbf(float x) { return bflo((unsigned)__builtin_bit_cast(u16, (__bf16)x)); }
template <int CTRL> __device__ __forceinline__ float dppf(float x) {
  return __builtin_bit_cast(float, __builtin_amdgcn_mov_dpp(__builtin_bit_cast(int, x), CTRL, 0xf, 0xf, true));
}
constexpr int XOR1 = 0xB1, XOR2 = 0x4E, XOR7 = 0x141, XOR8 = 0x128, UP4 = 0x104, DOWN4 = 0x114;
template <int N> __device__ __forceinline__ unsigned bcast16(unsigned x) {
  return (unsigned)__builtin_amdgcn_mov_dpp((int)x, 0x150 + N, 0xf, 0xf, true);
}
__device__ __forceinline__ float x16(float x, bool mx) {
  auto s = __builtin_amdgcn_permlane16_swap(__float_as_uint(x), __float_as_uint(x), false, false);
  x = mx ? fmaxf(__uint_as_float(s[0]), __uint_as_float(s[1])) : __uint_as_float(s[0]) + __uint_as_float(s[1]);
  auto t = __builtin_amdgcn_permlane32_swap(__float_as_uint(x), __float_as_uint(x), false, false);
  return mx ? fmaxf(__uint_as_float(t[0]), __uint_as_float(t[1])) : __uint_as_float(t[0]) + __uint_as_float(t[1]);
}
__device__ __forceinline__ float wave_sum(float v) {
  v += dppf<XOR1>(v); v += dppf<XOR2>(v); v += dppf<XOR7>(v); v += dppf<XOR8>(v); return x16(v, false);
}
__device__ __forceinline__ float wave_max(float v) {
  v = fmaxf(v, dppf<XOR1>(v)); v = fmaxf(v, dppf<XOR2>(v)); v = fmaxf(v, dppf<XOR7>(v)); v = fmaxf(v, dppf<XOR8>(v)); return x16(v, true);
}
__device__ __forceinline__ float hi_half(float x) {
  auto t = __builtin_amdgcn_permlane32_swap(__float_as_uint(x), __float_as_uint(x), false, false);
  return __uint_as_float(t[1]);
}
template <int CH, int CL> __device__ __forceinline__ float tmerge(float lo, float hi, bool b) {
  const float u = hi + dppf<CH>(hi), v = lo + dppf<CL>(lo);
  return b ? u : v;
}
__device__ __forceinline__ float reduce8(const f32x4& v, bool b3) {
  const float u = tmerge<XOR8, XOR8>(v[0], v[1], b3), t = tmerge<XOR8, XOR8>(v[2], v[3], b3);
  auto a = __builtin_amdgcn_permlane16_swap(__float_as_uint(u), __float_as_uint(t), false, false);
  const float z = __uint_as_float(a[0]) + __uint_as_float(a[1]);
  auto e = __builtin_amdgcn_permlane32_swap(__float_as_uint(z), __float_as_uint(z), false, false);
  return __uint_as_float(e[0]) + __uint_as_float(e[1]);
}
__device__ __forceinline__ void pin(float& x) { asm volatile("" : "+v"(x)); }
__device__ __forceinline__ f32x4 mfma16(u32x4 a, u32x4 b, f32x4 c) {
  return __builtin_amdgcn_mfma_f32_16x16x32_bf16(__builtin_bit_cast(bf16x8, a), __builtin_bit_cast(bf16x8, b), c, 0, 0, 0);
}
__device__ __forceinline__ float dot2(unsigned a, unsigned b, float c) {
  return __builtin_amdgcn_fdot2_f32_bf16(__builtin_bit_cast(bf16x2, a), __builtin_bit_cast(bf16x2, b), c, false);
}
__device__ __forceinline__ float silu(float x) { return x / (1.0f + __expf(-x)); }
__device__ __forceinline__ u32x4 diag(unsigned pk, const u32x4& sel) {
  u32x4 a;
  for (int i = 0; i < 4; ++i) a[i] = __builtin_amdgcn_perm(0u, pk, sel[i]);
  return a;
}
__device__ __forceinline__ auto rsrc(const void* p, int bytes) { return __builtin_amdgcn_make_buffer_rsrc((void*)p, 0, bytes, 0x00020000); }
constexpr int NT = 2, SC1 = 16;
__device__ __forceinline__ void glds16(const void* gsrc, unsigned lds_dst) {
  unsigned keep;
  asm volatile("s_mov_b32 %0, m0\n\ts_mov_b32 m0, %2\n\ts_nop 0\n\tglobal_load_lds_dwordx4 %1, off nt\n\ts_mov_b32 m0, %0" : "=&s"(keep) : "v"(gsrc), "s"(lds_dst) : "memory");
}
__device__ __forceinline__ u32x2 trread(LAS char* p) { return __builtin_bit_cast(u32x2, __builtin_amdgcn_ds_read_tr16_b64_v4i16((LAS s16x4*)p)); }

struct LnStat { float m, den, inv; };
__device__ __forceinline__ LnStat ln_finish(float mean, float var) {
  const float den = rbf(sqrtf(rbf(rbf(var) + 1e-5f)));
  return LnStat{rbf(mean), den, 1.0f / den};
}
__device__ __forceinline__ float ln_apply(float x, const LnStat& s) {
  const float a = rbf(x - s.m), q = a * s.inv;
  return fmaf(fmaf(-q, s.den, a), s.inv, q);
}

__global__ void __launch_bounds__(512, 2) k_main(const u16* __restrict__ x, const u16* __restrict__ K, const u16* __restrict__ V, const u16* __restrict__ wq,
                                                 const u16* __restrict__ wo, const u16* __restrict__ w1, const u16* __restrict__ w3,
                                                 const u16* __restrict__ w2, char* ws, u16* __restrict__ out) {
  __shared__ __attribute__((aligned(16))) char lds_[L_END];
  LAS char* lds = (LAS char*)lds_;
  const unsigned tent = now();
  asm volatile("" ::"s"(x), "s"(K), "s"(V), "s"(wq), "s"(w1), "s"(w3), "s"(w2), "s"(ws));
  const int tid = threadIdx.x, lane = tid & 63, w = __builtin_amdgcn_readfirstlane(tid >> 6);
  const int xg = blockIdx.x & 7, j0 = blockIdx.x >> 3, b = j0 >> 2;
  const int h = (j0 & 1) | (xg << 1) | ((j0 & 2) << 3), j = (j0 & 1) * 16 + (j0 >> 1);
  const char* Kb = (const char*)(K + (size_t)(b * NH + h) * SK * DH);
  const int voff = lane * 16 + w * 4096;
  u32x4 ring[16];
  auto ld = [&](const char* blk, auto ic) { constexpr int i = ic.v;
    ring[i] = __builtin_amdgcn_raw_buffer_load_b128(rsrc(blk, 65536), voff + (i & 3) * 1024, (i >> 2) * 16384, NT); };
  if (w < 4) sfor<PK0>([&](auto ic) { ld(Kb, ic); });
  SCHED();
  const int c = lane >> 4, n = lane & 15;
  volatile LAS unsigned* ctl = (volatile LAS unsigned*)(lds + L_CTL);
  if (tid < 8) ctl[tid] = 0u;
  __syncthreads();
  u32x4 sel;
  {
    const unsigned src = (n < 8) ? 0x0100u : 0x0302u; const int jj = n & 7;
    for (int i = 0; i < 4; ++i) sel[i] = ((jj >> 1) != i) ? 0x0c0c0c0cu : ((jj & 1) ? (0x0c0cu | (src << 16)) : (0x0c0c0000u | src));
  }
  if (w < 4) {
    const char* Vb = (const char*)(V + (size_t)(b * NH + h) * SK * DH);
    u32x4 pre[16 * NPRE];
    sfor<16 - PK0>([&](auto ic) { SCHED(); vmwait<PKW - 1>(); SCHED(); ld(Kb, IC<ic.v + PK0>{}); });
    sfor<16 * NPRE>([&](auto ic) { constexpr int i = ic.v; SCHED(); vmwait<PKW - 1>(); SCHED();
      pre[i] = __builtin_amdgcn_raw_buffer_load_b128(rsrc(Kb + 65536, NPRE * 65536), voff + (i & 3) * 1024, (i >> 2) * 16384, NT); });
    SCHED();
    __builtin_amdgcn_s_setprio(3);
    SPIN(LDS_CAP) { if (ctl[C_Q] >= 3u) break; __builtin_amdgcn_s_sleep(1); }
    asm volatile("" ::: "memory");
    unsigned qd[4];
    {
      f32x4 a = {0.f, 0.f, 0.f, 0.f}, e = a;
      for (int i = 0; i < 3; ++i) { a += *(LAS f32x4*)(lds + L_QP + i * 512 + 32 * n); e += *(LAS f32x4*)(lds + L_QP + i * 512 + 32 * n + 16); }
      qd[0] = pack2(a[0], a[1]); qd[1] = pack2(a[2], a[3]); qd[2] = pack2(e[0], e[1]); qd[3] = pack2(e[2], e[3]);
    }
    LAS u16* sc = (LAS u16*)(lds + L_SC) + w * 1024 + lane;
    float mx = -INFINITY;
    int lvl = PC_L0;
    unsigned due = tent + (PC_T3 - PC_RK);
    auto pace = [&](int ticks) {
      due += ticks;
      const int shift = __builtin_amdgcn_readfirstlane(ctl[C_SHIFT]);
      const int lag = min(max((int)(now() - due), -4096), 4096);
      if (w == 0) ctl[C_LAG] = lag;
      lvl = min(max(PC_L0 + ((lag - shift) * PC_G >> 6), 0), 48);
    };
    const bool b0 = n & 1, b1 = n & 2, b2 = n & 4, b3 = n & 8;
    auto kblock = [&](const char* cur, const char* nxt, int blk, auto mode) {
      float t0, t1, t2, t3;
      sfor<16>([&](auto ic) { constexpr int i = ic.v;
        const u32x4 kv = mode.v >= 2 ? pre[(mode.v >= 2 ? mode.v - 2 : 0) * 16 + i] : ring[i];
        if constexpr (mode.v == 0 || (mode.v == -1 && i + WIN >= XPRE)) {
          SCHED(); vmwait_l<i>(lvl); SCHED(); if constexpr (i + WIN >= 16) ld(nxt, IC<i + WIN - 16>{}); else ld(cur, IC<i + WIN>{}); SCHED(); }
        float d = dot2(kv[0], qd[0], 0.f); d = dot2(kv[1], qd[1], d); d = dot2(kv[2], qd[2], d); d = dot2(kv[3], qd[3], d);
        pin(d);
        SCHED();
        if constexpr (mode.v == 1 && i < XPRE) { ld(nxt, ic); SCHED(); }
        if constexpr (!(i & 1)) t0 = d;
        else {
          d = tmerge<XOR1, XOR1>(t0, d, b0);
          if constexpr (!(i & 2)) t1 = d;
          else {
            d = tmerge<XOR2, XOR2>(t1, d, b1);
            if constexpr (!(i & 4)) t2 = d;
            else {
              d = tmerge<DOWN4, UP4>(t2, d, b2);
              if constexpr (!(i & 8)) t3 = d;
              else {
                d = tmerge<XOR8, XOR8>(t3, d, b3);
                d = rbf(rbf(d) * 0.08838834764831845f);
                mx = fmaxf(mx, d);
                sc[blk * 64] = (u16)(__float_as_uint(d) >> 16);
                if (w == 0) ctl[C_BLK] = blk + 1;
              }
            }
          }
        }
      });
    };
    constexpr int B0 = NPRE + 1;
    kblock(Kb, Kb + B0 * 65536, 0, IC<1>{});
    sfor<NPRE>([&](auto pc) { kblock(Kb, Kb, 1 + pc.v, IC<2 + pc.v>{}); });
    kblock(Kb + B0 * 65536, Kb + (B0 + 1) * 65536, B0, IC<-1>{});
    pace(PC_RK);
#pragma unroll 1
    for (int blk = B0 + 1; blk < 16; ++blk) { kblock(Kb + blk * 65536, blk < 15 ? Kb + (blk + 1) * 65536 : Vb, blk, IC<0>{}); pace(PC_RK); }
    mx = wave_max(mx);
    float l = 0.f;
    f32x4 acc = {0.f, 0.f, 0.f, 0.f};
    float s = bflo(sc[0]);
    LAS float* mr = (LAS float*)(lds + L_MRG) + w * 132;
    auto vblock = [&](const char* cur, const char* nxt, int blk, auto last) {
      if (w == 0) ctl[C_BLK] = 16 + blk;
      const float p = __expf(s - mx);
      l += p;
      const float ph = rbf(p);
      const unsigned pk = pack2(ph, p - ph);
      if constexpr (!last.v) s = bflo(sc[(blk + 1) * 64]);
      else {
        l = wave_sum(l);
        if (lane == 0) { mr[0] = mx; mr[1] = l; }
        asm volatile("s_waitcnt lgkmcnt(0)" ::: "memory");
        if (lane == 0) __hip_atomic_fetch_add((LAS unsigned*)(lds + L_CTL) + C_ML, 1u, __ATOMIC_RELAXED, __HIP_MEMORY_SCOPE_WORKGROUP);
      }
      sfor<16>([&](auto ic) { constexpr int i = ic.v;
        const u32x4 a = diag(bcast16<i>(pk), sel), vv = ring[i];
        SCHED();
        if constexpr (!last.v || i + WIN < 16) vmwait_l<i>(lvl);
        SCHED();
        if constexpr (i + WIN < 16) ld(cur, IC<i + WIN>{}); else if constexpr (!last.v) ld(nxt, IC<i + WIN - 16>{});
        SCHED();
        acc = mfma16(a, vv, acc);
        SCHED();
      });
    };
#pragma unroll 1
    for (int blk = 0; blk < 15; ++blk) { vblock(Vb + blk * 65536, Vb + (blk + 1) * 65536, blk, IC<0>{}); pace(PC_RV); }
    vblock(Vb + 15 * 65536, Vb, 15, IC<1>{});
    for (int r = 0; r < 2; ++r) {
      auto t = __builtin_amdgcn_permlane32_swap(__float_as_uint(acc[r]), __float_as_uint(acc[r + 2]), false, false);
      mr[4 + (4 * (c & 1) + 2 * (c >> 1) + r) * 16 + n] = __uint_as_float(t[0]) + __uint_as_float(t[1]);
    }
    __syncthreads();
  } else if (w < 7) {
    const int i4 = w - 4;
    {
      const unsigned* xr = (const unsigned*)(x + b * DM);
      unsigned d[5];
      for (int t = 0; t < 4; ++t) d[t] = xr[lane + 64 * t];
      d[4] = xr[256 + (lane & 31)];
      const u16* xe = x + b * DM + 192 * i4 + c;
      const unsigned e0 = xe[8 * n], e1 = xe[8 * n + 4], e2 = xe[128 + 8 * (n & 7)], e3 = xe[128 + 8 * (n & 7) + 4];
      SCHED();
      u32x4 wr[48];
      const auto rq = rsrc(wq, DM * HD * 2);
      const int vo = (4 * (n >> 3) + c) * (HD * 2) + (n & 7) * 16;
      const int so = 192 * i4 * (HD * 2) + h * 256;
      auto ldq = [&](auto ic) { constexpr int i = ic.v;
        if constexpr (i < 48) wr[i] = __builtin_amdgcn_raw_buffer_load_b128(rq, vo + (i & 1) * 128, so + (i >> 1) * 8 * (HD * 2), NT); };
      sfor<PQW>(ldq);
      SCHED();
      float sm = 0.f, sq = 0.f;
      for (int t = 0; t < 5; ++t) sm += (t < 4 || lane < 32) ? bflo(d[t]) + bfhi(d[t]) : 0.f;
      const float mean = wave_sum(sm) * (1.0f / DM);
      for (int t = 0; t < 5; ++t) { const float a = bflo(d[t]) - mean, e = bfhi(d[t]) - mean; sq += (t < 4 || lane < 32) ? a * a + e * e : 0.f; }
      const LnStat st = ln_finish(mean, wave_sum(sq) * (1.0f / DM));
      const unsigned pk0 = pack2(ln_apply(bflo(e0), st), ln_apply(bflo(e1), st)), pk1 = pack2(ln_apply(bflo(e2), st), ln_apply(bflo(e3), st));
      f32x4 a0 = {0.f, 0.f, 0.f, 0.f}, a1 = a0;
      sfor<24>([&](auto sc_) { constexpr int s = sc_.v;
        const u32x4 a = diag(bcast16<s & 15>(s < 16 ? pk0 : pk1), sel);
        a0 = mfma16(a, wr[2 * s], a0); SCHED(); ldq(IC<2 * s + PQW>{}); SCHED();
        a1 = mfma16(a, wr[2 * s + 1], a1); SCHED(); ldq(IC<2 * s + 1 + PQW>{}); SCHED(); });
      f32x4 o0, o1;
      for (int r = 0; r < 4; ++r) { o0[r] = a0[r] + hi_half(dppf<XOR8>(a0[r])); o1[r] = a1[r] + hi_half(dppf<XOR8>(a1[r])); }
      if (c < 2 && n < 8) { *(LAS f32x4*)(lds + L_QP + i4 * 512 + 32 * n + 16 * c) = o0; *(LAS f32x4*)(lds + L_QP + i4 * 512 + 256 + 32 * n + 16 * c) = o1; }
      asm volatile("s_waitcnt lgkmcnt(0)" ::: "memory");
      if (lane == 0) __hip_atomic_fetch_add((LAS unsigned*)(lds + L_CTL) + C_Q, 1u, __ATOMIC_RELAXED, __HIP_MEMORY_SCOPE_WORKGROUP);
    }
    u32x4 wr[48];
    const auto ro = rsrc(wo, HD * DM * 2);
    const int vo = (4 * (n >> 3) + c) * (DM * 2) + (n & 7) * 16;
    const int so = 128 * h * (DM * 2) + 384 * i4;
    sfor<48>([&](auto ic) { constexpr int i = ic.v;
      SPIN(LDS_CAP) { if (now() - tent >= (unsigned)(WO_T0 + i * WO_DT)) break; __builtin_amdgcn_s_sleep(2); }
      wr[i] = __builtin_amdgcn_raw_buffer_load_b128(ro, vo + (i % 3) * 128, so + (i / 3) * 8 * (DM * 2), NT);
      SCHED(); vmwait<1>(); SCHED(); });
    const LAS float* mr = (const LAS float*)(lds + L_MRG);
    SPIN(LDS_CAP) { if (ctl[C_ML] >= 4u) break; __builtin_amdgcn_s_sleep(4); }
    asm volatile("" ::: "memory");
    float f4[4], M = -INFINITY, L = 0.f;
    for (int i = 0; i < 4; ++i) { f4[i] = mr[i * 132]; M = fmaxf(M, f4[i]); }
    for (int i = 0; i < 4; ++i) { f4[i] = __expf(f4[i] - M); L += f4[i] * mr[i * 132 + 1]; }
    float linv = 1.0f / L;
    for (int i = 0; i < 4; ++i) pin(f4[i]);
    pin(linv);
    __syncthreads();
    float oa = 0.f, ob = 0.f;
    for (int i = 0; i < 4; ++i) { oa += f4[i] * mr[i * 132 + 4 + c * 16 + n]; ob += f4[i] * mr[i * 132 + 4 + (4 + c) * 16 + n]; }
    const unsigned pk = pack2(oa * linv, ob * linv);
    f32x4 a3[3] = {{0.f, 0.f, 0.f, 0.f}, {0.f, 0.f, 0.f, 0.f}, {0.f, 0.f, 0.f, 0.f}};
    sfor<16>([&](auto sc_) { constexpr int s = sc_.v;
      const u32x4 a = diag(bcast16<s>(pk), sel);
      for (int t = 0; t < 3; ++t) a3[t] = mfma16(a, wr[3 * s + t], a3[t]); });
    const int po = WS_P + ((b * NH + h) * DM + 192 * i4 + 8 * n + 4 * (c & 1)) * 4;
    u32x4 o01, o2;
    for (int r = 0; r < 4; ++r) {
      auto t = __builtin_amdgcn_permlane32_swap(__float_as_uint(a3[0][r]), __float_as_uint(a3[1][r]), false, false);
      const unsigned v = __float_as_uint(__uint_as_float(t[0]) + dppf<XOR8>(__uint_as_float(t[1])));
      o01[r] = v == SENT ? SENT - 1u : v;
    }
    if (n < 8) __builtin_amdgcn_raw_buffer_store_b128(o01, rsrc(ws, WS_END), po + 256 * (c >> 1), 0, SC1);
    for (int r = 0; r < 4; ++r) {
      const unsigned v = __float_as_uint(a3[2][r] + hi_half(dppf<XOR8>(a3[2][r])));
      o2[r] = v == SENT ? SENT - 1u : v;
    }
    if (n < 8 && c < 2) __builtin_amdgcn_raw_buffer_store_b128(o2, rsrc(ws, WS_END), po + 512, 0, SC1);
  } else {
    const auto rw = rsrc(ws, WS_END);
    auto gate = [&](unsigned blocks) { SPIN(LDS_CAP) { if (ctl[C_BLK] >= blocks) break; __builtin_amdgcn_s_sleep(8); } };
    const bool gu = j < 18, dn = j >= 18 && j < 27;
    const int i0 = (14 - xg) & 7, iu = j >= 16 ? i0 : (j >> 1) + ((j >> 1) >= i0);
    const int mat = j & 1, u = 9 * xg + iu, hb = j - 18;
    gu32* flag = (gu32*)(ws + (gu ? WS_FG + (2 * u + mat) * 4 : WS_FP + (xg * 9 + hb) * 4));
    if (j < 27) {
      const int ldw = gu ? FF * 2 : DM * 2;
      const char* src = (gu ? (const char*)((mat ? w3 : w1) + 64 * u) : (const char*)(w2 + (size_t)576 * xg * DM + 64 * hb)) + (lane >> 3) * ldw + (lane & 7) * 16;
#pragma unroll 1
      for (int i = 0; i < 72; ++i) { glds16(src, (unsigned)(size_t)(lds + L_W) + i * 1024); src += 8 * ldw; vmwait<FFW - 1>(); }
    }
    auto echo_of = [](unsigned long long y) { return (unsigned)y & 0xffffffu; };
    auto chal_of = [](unsigned long long y) { return (unsigned)(y >> 24) & 0xffffffu; };
    auto word = [](unsigned echo, unsigned ch, unsigned hint) {
      return (unsigned long long)(echo & 0xffffffu) | ((unsigned long long)ch << 24) | ((unsigned long long)(hint & 0xffffu) << 48); };
    const int ri = j0 == 2 ? 0 : j0 == 12 ? 1 : j0 == 23 ? 2 : j0 == 29 ? 3 : -1;
    gu64* hi = (gu64*)(ws + WS_HI); gu64* ho = (gu64*)(ws + WS_HO);
    const unsigned long long yo = __hip_atomic_load(ho + blockIdx.x, RLX_AGENT), yi = __hip_atomic_load(hi + blockIdx.x, RLX_AGENT);
    {
      const u32x4 e = {SENT, SENT, SENT, SENT};
      for (int t = 0; t < 3; ++t)
        if (t < 2 || lane < 16) __builtin_amdgcn_raw_buffer_store_b128(e, rw, WS_P + (b * NH + h) * DM * 4 + (t * 64 + lane) * 16, 0, SC1);
      if (lane == 0 && j < 27) __hip_atomic_store(flag, 0u, RLX_AGENT);
      if (ri >= 0 && lane < NRND) __hip_atomic_store((gu32*)(ws + WS_RP + lane * 128 + (xg * 4 + ri) * 4), 0u, RLX_AGENT);
      if (blockIdx.x == LEADER && lane < NRND * 8) __hip_atomic_store((gu32*)(ws + WS_BC + lane * 128), 0u, RLX_AGENT);
    }
    const unsigned f0 = echo_of(yo), old = chal_of(yi);
    const unsigned chal = (f0 + (((old - f0 - 1u) & 0xffffffu) < 2u ? 3u : 1u)) & 0xffffffu;
    auto tile = [&](f32x4& r0, f32x4& r1) {
      const int q = n >> 2, p = n & 3;
      LAS char* wp = lds + L_W + (8 * c + q) * 128 + (gu ? 16 * p : 8 * p);
      const int o1 = gu ? 8 : 32, o2 = 64;
      const LAS char* ap = lds + L_ACT + (n & 7) * ACT_LD + 16 * c;
      f32x4 d[4];
      for (int T = 0; T < 4; ++T) d[T] = f32x4{0.f, 0.f, 0.f, 0.f};
#pragma unroll 2
      for (int s = 0; s < 18; ++s) {
        const u32x4 av = *(const LAS u32x4*)(ap + 64 * s);
        for (int T = 0; T < 4; ++T) {
          LAS char* a = wp + s * 4096 + (T & 1) * o1 + (T >> 1) * o2;
          const u32x2 lo = trread(a), hi2 = trread(a + 512);
          d[T] = mfma16(u32x4{lo[0], lo[1], hi2[0], hi2[1]}, av, d[T]);
        }
      }
      if (gu) { r0 = n < 8 ? d[0] : d[2]; r1 = n < 8 ? d[1] : d[3]; }
      else { r0 = n < 8 ? d[0] : d[1]; r1 = n < 8 ? d[2] : d[3]; }
    };
    u32x4 xd[9];
    if (gu) for (int t = 0; t < 9; ++t) xd[t] = *(const u32x4*)(x + (lane >> 3) * DM + 8 * ((lane & 7) + 8 * t));
    vmwait<0>();
    if (blockIdx.x != LEADER && lane == 0) __hip_atomic_store(hi + blockIdx.x, word(chal_of(yo), chal, tent), RLX_AGENT);
    if (ECHO_FIRST && blockIdx.x != LEADER) {
      gate(G_HS);
      SPIN(SPIN_CAP) {
        const unsigned long long y = __hip_atomic_load(ho + blockIdx.x, RLX_AGENT);
        if (((__builtin_amdgcn_readfirstlane(echo_of(y)) - chal) & 0xffffffu) < 2u) {
          if (lane == 0) __hip_atomic_store(hi + blockIdx.x, word(chal_of(y), chal, ctl[C_LAG] + tent), RLX_AGENT);
          break;
        }
        __builtin_amdgcn_s_sleep(64);
      }
    }
    if (gu) {
      float sm = 0.f, sq = 0.f;
      for (int t = 0; t < 9; ++t) for (int e = 0; e < 4; ++e) sm += bflo(xd[t][e]) + bfhi(xd[t][e]);
      sm += dppf<XOR1>(sm); sm += dppf<XOR2>(sm); sm += dppf<XOR7>(sm);
      const float mean = sm * (1.0f / DM);
      for (int t = 0; t < 9; ++t) for (int e = 0; e < 4; ++e) { const float a = bflo(xd[t][e]) - mean, d = bfhi(xd[t][e]) - mean; sq += a * a + d * d; }
      sq += dppf<XOR1>(sq); sq += dppf<XOR2>(sq); sq += dppf<XOR7>(sq);
      const LnStat st = ln_finish(mean, sq * (1.0f / DM));
      for (int t = 0; t < 9; ++t) {
        u32x4 v;
        for (int e = 0; e < 4; ++e) v[e] = pack2(ln_apply(bflo(xd[t][e]), st), ln_apply(bfhi(xd[t][e]), st));
        *(LAS u32x4*)(lds + L_ACT + (lane >> 3) * ACT_LD + 16 * ((lane & 7) + 8 * t)) = v;
      }
      f32x4 r0, r1;
      tile(r0, r1);
      const u32x4 o = {pack2(r0[0], r0[1]), pack2(r0[2], r0[3]), pack2(r1[0], r1[1]), pack2(r1[2], r1[3])};
      __builtin_amdgcn_raw_buffer_store_b128(o, rw, WS_GU + ((mat * NB + (n & 7)) * FF + 64 * u + 32 * (n >> 3) + 8 * c) * 2, 0, SC1);
      vmwait<0>();
      if (lane == 0) __hip_atomic_store(flag, 1u, RLX_AGENT);
    }
    if (blockIdx.x == LEADER) {
      unsigned long long y[4]; unsigned cl[4], sent[4];
      gate(G_L1);
      for (int q = 0; q < 4; ++q) { y[q] = __hip_atomic_load(hi + q * 64 + lane, RLX_AGENT); cl[q] = (echo_of(y[q]) + 1u) & 0xffffffu; sent[q] = ~0u; }
      SPIN(SPIN_CAP) {
        bool ok = true;
        for (int q = 0; q < 4; ++q) ok = ok && (echo_of(y[q]) == cl[q] || q * 64 + lane == LEADER);
        if (__all(ok)) break;
        for (int q = 0; q < 4; ++q)
          if (sent[q] != chal_of(y[q])) { sent[q] = chal_of(y[q]); __hip_atomic_store(ho + q * 64 + lane, word(sent[q], cl[q], 0u), RLX_AGENT); }
        gate(spins == 0 ? G_L1 + 1 : G_L2);
        __builtin_amdgcn_s_sleep(32);
        for (int q = 0; q < 4; ++q) y[q] = __hip_atomic_load(hi + q * 64 + lane, RLX_AGENT);
      }
      float ls = 0.f;
      for (int q = 0; q < 4; ++q) ls += q * 64 + lane == LEADER ? 0.f : (float)(short)((unsigned)(y[q] >> 48) - tent);
      const int mean = (int)(wave_sum(ls) * (1.0f / 255.f));
      for (int q = 0; q < 4; ++q) __hip_atomic_store(ho + q * 64 + lane, word(chal_of(y[q]) + 1u, cl[q], mean + tent), RLX_AGENT);
      ctl[C_SHIFT] = mean;
    } else {
      if (!ECHO_FIRST) {
      gate(G_HS);
      SPIN(SPIN_CAP) {
        const unsigned long long y = __hip_atomic_load(ho + blockIdx.x, RLX_AGENT);
        if (((__builtin_amdgcn_readfirstlane(echo_of(y)) - chal) & 0xffffffu) < 2u) {
          if (lane == 0) __hip_atomic_store(hi + blockIdx.x, word(chal_of(y), chal, ctl[C_LAG] + tent), RLX_AGENT);
          break;
        }
        __builtin_amdgcn_s_sleep(64);
      }
      }
      gate(G_GO);
      SPIN(SPIN_CAP) {
        const unsigned long long y = __hip_atomic_load(ho + blockIdx.x, RLX_AGENT);
        if (__builtin_amdgcn_readfirstlane(echo_of(y)) == ((chal + 1u) & 0xffffffu)) { ctl[C_SHIFT] = (int)(short)((unsigned)(y >> 48) - tent); break; }
        __builtin_amdgcn_s_sleep(64);
      }
    }
    asm volatile("" ::: "memory");
    if (dn) {
      gate(G_D);
      gu32* fg = (gu32*)(ws + WS_FG) + 18 * xg + (lane < 18 ? lane : 0);
      SPIN(SPIN_CAP) { if (__all(__hip_atomic_load(fg, RLX_AGENT) == 1u)) break; __builtin_amdgcn_s_sleep(64); }
      asm volatile("" ::: "memory");
#pragma unroll 1
      for (int t3 = 0; t3 < 3; ++t3) {
        u32x4 gv[3], uv[3]; int lo[3];
        for (int t = 0; t < 3; ++t) {
          const int q = lane + 64 * (3 * t3 + t), bb = q / 72, ch = q - 72 * bb, go = WS_GU + (bb * FF + 576 * xg + 8 * ch) * 2;
          gv[t] = __builtin_amdgcn_raw_buffer_load_b128(rw, go, 0, SC1); uv[t] = __builtin_amdgcn_raw_buffer_load_b128(rw, go + NB * FF * 2, 0, SC1);
          lo[t] = L_ACT + bb * ACT_LD + 16 * ch;
        }
        for (int t = 0; t < 3; ++t) {
          u32x4 hv;
          for (int e = 0; e < 4; ++e) hv[e] = pack2(rbf(silu(bflo(gv[t][e]))) * bflo(uv[t][e]), rbf(silu(bfhi(gv[t][e]))) * bfhi(uv[t][e]));
          *(LAS u32x4*)(lds + lo[t]) = hv;
        }
      }
      f32x4 r0, r1;
      tile(r0, r1);
      const int fo = WS_PF + ((xg * NB + (n & 7)) * DM + 64 * hb + 16 * (n >> 3) + 4 * c) * 4;
      __builtin_amdgcn_raw_buffer_store_b128(__builtin_bit_cast(u32x4, r0), rw, fo, 0, SC1);
      __builtin_amdgcn_raw_buffer_store_b128(__builtin_bit_cast(u32x4, r1), rw, fo + 128, 0, SC1);
      vmwait<0>();
      if (lane == 0) __hip_atomic_store(flag, 1u, RLX_AGENT);
    }
    const int cons = (h & 1) == 0 ? (h >> 1) : (h < 4 ? 16 + (h >> 1) : -1);
    const int pl = lane >> 3, col = 32 * cons + 4 * (lane & 7);
    const int ocol = b * DM + col + 2 * (c & 1) + (n >> 3);
    float xx = 0.f, gg = 0.f;
#pragma unroll 1
    for (int r = 0; r < NRND; ++r) {
      const unsigned blk = RND0 + RNDS * r;
      gate(blk);
      if (ri >= 0 && lane == 0) __hip_atomic_store((gu32*)(ws + WS_RP + r * 128 + (xg * 4 + ri) * 4), ((ctl[C_LAG] + tent) & 0xffffu) | 0x10000u, RLX_AGENT);
      if (blockIdx.x == LEADER) {
        gate(blk + 1);
        const unsigned v = __hip_atomic_load((gu32*)(ws + WS_RP + r * 128 + (lane & 31) * 4), RLX_AGENT);
        const float cnt = wave_sum(v != 0u ? 1.f : 0.f), sum = wave_sum(v != 0u ? (float)(short)(v - tent) : 0.f);
        if (cnt >= 32.f && lane < 8) __hip_atomic_store((gu32*)(ws + WS_BC + (r * 8 + lane) * 128), (((int)(sum / cnt) + tent) & 0xffffu) | 0x10000u, RLX_AGENT);
      }
      if (r == RND_F && cons >= 0) {
        xx = bflo(x[ocol]);
        gu32* fp = (gu32*)(ws + WS_FP) + 9 * pl + (cons >> 1);
        SPIN(SPIN_CAP) { if (__all(__hip_atomic_load(fp, RLX_AGENT) == 1u)) break; __builtin_amdgcn_s_sleep(64); }
        asm volatile("" ::: "memory");
        gg = rbf(silu(rbf(reduce8(__builtin_bit_cast(f32x4, __builtin_amdgcn_raw_buffer_load_b128(rw, WS_PF + ((pl * NB + b) * DM + col) * 4, 0, SC1)), n & 8))));
      }
      gate(blk + 2);
      const unsigned m = __hip_atomic_load((gu32*)(ws + WS_BC + (r * 8 + xg) * 128), RLX_AGENT);
      if (m != 0u) ctl[C_SHIFT] = (int)(short)(m - tent);
    }
    if (cons < 0) { __syncthreads(); return; }
    __syncthreads();
    __builtin_amdgcn_s_sleep(POLL_DELAY);
    u32x4 pv[4];
    int po[4];
    for (int t = 0; t < 4; ++t) { pv[t] = u32x4{SENT, SENT, SENT, SENT}; po[t] = WS_P + ((b * NH + 8 * t + pl) * DM + col) * 4; }
    SPIN(16 * SPIN_CAP) {
      asm volatile("" ::: "memory");
      u32x4 v[4];
      for (int t = 0; t < 4; ++t) v[t] = __builtin_amdgcn_raw_buffer_load_b128(rw, po[t], 0, SC1);
      bool ok = true;
      for (int t = 0; t < 4; ++t) {
        const bool had = po[t] == WS_END, got = v[t][0] != SENT && v[t][1] != SENT && v[t][2] != SENT && v[t][3] != SENT;
        for (int r = 0; r < 4; ++r) pv[t][r] = had ? pv[t][r] : v[t][r];
        po[t] = got ? WS_END : po[t];
        ok = ok && got;
      }
      if (__all(ok)) break;
    }
    f32x4 s4;
    for (int r = 0; r < 4; ++r) s4[r] = (__uint_as_float(pv[0][r]) + __uint_as_float(pv[1][r])) + (__uint_as_float(pv[2][r]) + __uint_as_float(pv[3][r]));
    const float o = xx + rbf(rbf(reduce8(s4, n & 8)) * gg);
    if (lane < 32) out[ocol] = (u16)pack2(o, 0.f);
  }
}

extern "C" void kernel_launch(void* const* d_in, const int* in_sizes, int n_in, void* d_out, int out_size, void* d_ws, size_t ws_size, hipStream_t stream) {
  k_main<<<256, 512, 0, stream>>>((const u16*)d_in[0], (const u16*)d_in[1], (const u16*)d_in[2], (const u16*)d_in[3], (const u16*)d_in[4], (const u16*)d_in[6],
                                  (const u16*)d_in[7], (const u16*)d_in[8], (char*)d_ws, (u16*)d_out);
}
```

```cpp
#include <hip/hip_runtime.h>
#include <stdint.h>

typedef unsigned short u16;
typedef unsigned u32x4 __attribute__((ext_vector_type(4)));
typedef unsigned u32x2 __attribute__((ext_vector_type(2)));
typedef float f32x4 __attribute__((ext_vector_type(4)));
typedef short s16x4 __attribute__((ext_vector_type(4)));
typedef __bf16 bf16x8 __attribute__((ext_vector_type(8)));
typedef __bf16 bf16x2 __attribute__((ext_vector_type(2)));
#define LAS __attribute__((address_space(3)))
typedef __attribute__((address_space(1))) unsigned gu32;
typedef __attribute__((address_space(1))) unsigned long long gu64;
#define RLX_AGENT __ATOMIC_RELAXED, __HIP_MEMORY_SCOPE_AGENT

constexpr int NB = 8, DM = 576, NH = 32, SK = 4096, DH = 128, FF = 4608, HD = NH * DH;

constexpr int PK0 = 6, PKW = 4, PQW = 5, FFW = 7;
constexpr int XPRE = 11;
constexpr int WO_T0 = 1500, WO_DT = 110;
constexpr int PC_T3 = 1385, PC_RK = 225, PC_RV = 221;
constexpr int PC_L0 = 19, PC_G = 12;
constexpr int G_L1 = 5, G_HS = 7, G_L2 = 8, G_GO = 10, G_D = 11;
constexpr int NRND = 5, RND0 = 11, RNDS = 4, RND_F = 2;
constexpr int POLL_DELAY = 40;
constexpr int WIN = 7;
constexpr int NPRE = 2;

constexpr int WS_P = 0, WS_PF = WS_P + NB * NH * DM * 4, WS_GU = WS_PF + 8 * NB * DM * 4, WS_FG = WS_GU + 2 * NB * FF * 2, WS_FP = WS_FG + 1024,
              WS_HI = WS_FP + 1024, WS_HO = WS_HI + 256 * 8, WS_RP = WS_HO + 256 * 8, WS_BC = WS_RP + NRND * 128,
              WS_END = WS_BC + NRND * 8 * 128;
constexpr int LEADER = 255;
constexpr unsigned SENT = 0xffffffffu;
constexpr unsigned SPIN_CAP = 1u << 18, LDS_CAP = 1u << 22;
constexpr int ACT_LD = DM * 2 + 16;
constexpr int L_W = 0;
constexpr int L_ACT = L_W + DM * 128;
constexpr int L_SC = L_ACT + NB * ACT_LD;
constexpr int L_QP = L_SC + 4 * 16 * 64 * 2;
constexpr int L_MRG = L_QP + 3 * 128 * 4;
constexpr int L_CTL = L_MRG + 4 * 132 * 4;
constexpr int L_END = L_CTL + 32;
enum { C_Q = 0, C_BLK = 1, C_ML = 2, C_LAG = 3, C_SHIFT = 4 };

template <int I> struct IC { static constexpr int v = I; };
template <int N, int I = 0, typename Fn> __device__ __forceinline__ void sfor(Fn&& f) {
  if constexpr (I < N) { f(IC<I>{}); sfor<N, I + 1>(f); }
}
#define SCHED() __builtin_amdgcn_sched_barrier(0)
#define SPIN(cap) _Pragma("unroll 1") for (unsigned spins = 0; spins < (cap); ++spins)
template <int N> __device__ __forceinline__ void vmwait() { asm volatile("s_waitcnt vmcnt(%0)" ::"n"(N) : "memory"); }
template <int I> __device__ __forceinline__ void vmwait_l(int lvl) {
  constexpr int br = ((I & 1) << 3) | ((I & 2) << 1) | ((I & 4) >> 1) | ((I & 8) >> 3);
  asm volatile("s_cmp_gt_i32 %0, %1\n\ts_cbranch_scc1 3f\n\ts_cmp_gt_i32 %0, %2\n\ts_cbranch_scc1 2f\n\ts_cmp_gt_i32 %0, %3\n\ts_cbranch_scc1 1f\n\t"
               "s_waitcnt vmcnt(3)\n1:\n\ts_waitcnt vmcnt(4)\n2:\n\ts_waitcnt vmcnt(5)\n3:\n\ts_waitcnt vmcnt(6)"
               ::"s"(lvl), "n"(32 + br), "n"(16 + br), "n"(br) : "memory", "scc");
}
__device__ __forceinline__ unsigned now() { return (unsigned)__builtin_amdgcn_s_memrealtime(); }

__device__ __forceinline__ float bflo(unsigned x) { return __uint_as_float(x << 16); }
__device__ __forceinline__ float bfhi(unsigned x) { return __uint_as_float(x & 0xffff0000u); }
__device__ __forceinline__ unsigned pack2(float lo, float hi) { bf16x2 v = {(__bf16)lo, (__bf16)hi}; return __builtin_bit_cast(unsigned, v); }
__device__ __forceinline__ float rbf(float x) { return bflo((unsigned)__builtin_bit_cast(u16, (__bf16)x)); }
template <int CTRL> __device__ __forceinline__ float dppf(float x) {
  return __builtin_bit_cast(float, __builtin_amdgcn_mov_dpp(__builtin_bit_cast(int, x), CTRL, 0xf, 0xf, true));
}
constexpr int XOR1 = 0xB1, XOR2 = 0x4E, XOR7 = 0x141, XOR8 = 0x128, UP4 = 0x104, DOWN4 = 0x114;
template <int N> __device__ __forceinline__ unsigned bcast16(unsigned x) {
  return (unsigned)__builtin_amdgcn_mov_dpp((int)x, 0x150 + N, 0xf, 0xf, true);
}
__device__ __forceinline__ float x16(float x, bool mx) {
  auto s = __builtin_amdgcn_permlane16_swap(__float_as_uint(x), __float_as_uint(x), false, false);
  x = mx ? fmaxf(__uint_as_float(s[0]), __uint_as_float(s[1])) : __uint_as_float(s[0]) + __uint_as_float(s[1]);
  auto t = __builtin_amdgcn_permlane32_swap(__float_as_uint(x), __float_as_uint(x), false, false);
  return mx ? fmaxf(__uint_as_float(t[0]), __uint_as_float(t[1])) : __uint_as_float(t[0]) + __uint_as_float(t[1]);
}
__device__ __forceinline__ float wave_sum(float v) {
  v += dppf<XOR1>(v); v += dppf<XOR2>(v); v += dppf<XOR7>(v); v += dppf<XOR8>(v); return x16(v, false);
}
__device__ __forceinline__ float wave_max(float v) {
  v = fmaxf(v, dppf<XOR1>(v)); v = fmaxf(v, dppf<XOR2>(v)); v = fmaxf(v, dppf<XOR7>(v)); v = fmaxf(v, dppf<XOR8>(v)); return x16(v, true);
}
__device__ __forceinline__ float hi_half(float x) {
  auto t = __builtin_amdgcn_permlane32_swap(__float_as_uint(x), __float_as_uint(x), false, false);
  return __uint_as_float(t[1]);
}
template <int CH, int CL> __device__ __forceinline__ float tmerge(float lo, float hi, bool b) {
  const float u = hi + dppf<CH>(hi), v = lo + dppf<CL>(lo);
  return b ? u : v;
}
__device__ __forceinline__ float reduce8(const f32x4& v, bool b3) {
  const float u = tmerge<XOR8, XOR8>(v[0], v[1], b3), t = tmerge<XOR8, XOR8>(v[2], v[3], b3);
  auto a = __builtin_amdgcn_permlane16_swap(__float_as_uint(u), __float_as_uint(t), false, false);
  const float z = __uint_as_float(a[0]) + __uint_as_float(a[1]);
  auto e = __builtin_amdgcn_permlane32_swap(__float_as_uint(z), __float_as_uint(z), false, false);
  return __uint_as_float(e[0]) + __uint_as_float(e[1]);
}
__device__ __forceinline__ void pin(float& x) { asm volatile("" : "+v"(x)); }
__device__ __forceinline__ f32x4 mfma16(u32x4 a, u32x4 b, f32x4 c) {
  return __builtin_amdgcn_mfma_f32_16x16x32_bf16(__builtin_bit_cast(bf16x8, a), __builtin_bit_cast(bf16x8, b), c, 0, 0, 0);
}
__device__ __forceinline__ float dot2(unsigned a, unsigned b, float c) {
  return __builtin_amdgcn_fdot2_f32_bf16(__builtin_bit_cast(bf16x2, a), __builtin_bit_cast(bf16x2, b), c, false);
}
__device__ __forceinline__ float silu(float x) { return x / (1.0f + __expf(-x)); }
__device__ __forceinline__ u32x4 diag(unsigned pk, const u32x4& sel) {
  u32x4 a;
  for (int i = 0; i < 4; ++i) a[i] = __builtin_amdgcn_perm(0u, pk, sel[i]);
  return a;
}
__device__ __forceinline__ auto rsrc(const void* p, int bytes) { return __builtin_amdgcn_make_buffer_rsrc((void*)p, 0, bytes, 0x00020000); }
constexpr int NT = 2, SC1 = 16;
__device__ __forceinline__ void glds16(const void* gsrc, unsigned lds_dst) {
  unsigned keep;
  asm volatile("s_mov_b32 %0, m0\n\ts_mov_b32 m0, %2\n\ts_nop 0\n\tglobal_load_lds_dwordx4 %1, off nt\n\ts_mov_b32 m0, %0" : "=&s"(keep) : "v"(gsrc), "s"(lds_dst) : "memory");
}
__device__ __forceinline__ u32x2 trread(LAS char* p) { return __builtin_bit_cast(u32x2, __builtin_amdgcn_ds_read_tr16_b64_v4i16((LAS s16x4*)p)); }

struct LnStat { float m, den, inv; };
__device__ __forceinline__ LnStat ln_finish(float mean, float var) {
  const float den = rbf(sqrtf(rbf(rbf(var) + 1e-5f)));
  return LnStat{rbf(mean), den, 1.0f / den};
}
__device__ __forceinline__ float ln_apply(float x, const LnStat& s) {
  const float a = rbf(x - s.m), q = a * s.inv;
  return fmaf(fmaf(-q, s.den, a), s.inv, q);
}

__global__ void __launch_bounds__(512, 2) k_main(const u16* __restrict__ x, const u16* __restrict__ K, const u16* __restrict__ V, const u16* __restrict__ wq,
                                                 const u16* __restrict__ wo, const u16* __restrict__ w1, const u16* __restrict__ w3,
                                                 const u16* __restrict__ w2, char* ws, u16* __restrict__ out) {
  __shared__ __attribute__((aligned(16))) char lds_[L_END];
  LAS char* lds = (LAS char*)lds_;
  const unsigned tent = now();
  asm volatile("" ::"s"(x), "s"(K), "s"(V), "s"(wq), "s"(w1), "s"(w3), "s"(w2), "s"(ws));
  const int tid = threadIdx.x, lane = tid & 63, w = __builtin_amdgcn_readfirstlane(tid >> 6);
  const int xg = blockIdx.x & 7, j0 = blockIdx.x >> 3, b = j0 >> 2;
  const int h = (j0 & 1) | (xg << 1) | ((j0 & 2) << 3), j = (j0 & 1) * 16 + (j0 >> 1);
  const char* Kb = (const char*)(K + (size_t)(b * NH + h) * SK * DH);
  const int voff = lane * 16 + w * 4096;
  u32x4 ring[16];
  auto ld = [&](const char* blk, auto ic) { constexpr int i = ic.v;
    ring[i] = __builtin_amdgcn_raw_buffer_load_b128(rsrc(blk, 65536), voff + (i & 3) * 1024, (i >> 2) * 16384, NT); };
  if (w < 4) sfor<PK0>([&](auto ic) { ld(Kb, ic); });
  SCHED();
  const int c = lane >> 4, n = lane & 15;
  volatile LAS unsigned* ctl = (volatile LAS unsigned*)(lds + L_CTL);
  if (tid < 8) ctl[tid] = 0u;
  __syncthreads();
  u32x4 sel;
  {
    const unsigned src = (n < 8) ? 0x0100u : 0x0302u; const int jj = n & 7;
    for (int i = 0; i < 4; ++i) sel[i] = ((jj >> 1) != i) ? 0x0c0c0c0cu : ((jj & 1) ? (0x0c0cu | (src << 16)) : (0x0c0c0000u | src));
  }
  if (w < 4) {
    const char* Vb = (const char*)(V + (size_t)(b * NH + h) * SK * DH);
    u32x4 pre[16 * NPRE];
    sfor<16 - PK0>([&](auto ic) { SCHED(); vmwait<PKW - 1>(); SCHED(); ld(Kb, IC<ic.v + PK0>{}); });
    sfor<16 * NPRE>([&](auto ic) { constexpr int i = ic.v; SCHED(); vmwait<PKW - 1>(); SCHED();
      pre[i] = __builtin_amdgcn_raw_buffer_load_b128(rsrc(Kb + 65536, NPRE * 65536), voff + (i & 3) * 1024, (i >> 2) * 16384, NT); });
    SCHED();
    __builtin_amdgcn_s_setprio(3);
    SPIN(LDS_CAP) { if (ctl[C_Q] >= 3u) break; __builtin_amdgcn_s_sleep(1); }
    asm volatile("" ::: "memory");
    unsigned qd[4];
    {
      f32x4 a = {0.f, 0.f, 0.f, 0.f}, e = a;
      for (int i = 0; i < 3; ++i) { a += *(LAS f32x4*)(lds + L_QP + i * 512 + 32 * n); e += *(LAS f32x4*)(lds + L_QP + i * 512 + 32 * n + 16); }
      qd[0] = pack2(a[0], a[1]); qd[1] = pack2(a[2], a[3]); qd[2] = pack2(e[0], e[1]); qd[3] = pack2(e[2], e[3]);
    }
    LAS u16* sc = (LAS u16*)(lds + L_SC) + w * 1024 + lane;
    float mx = -INFINITY;
    int lvl = PC_L0;
    unsigned due = tent + (PC_T3 - PC_RK);
    auto pace = [&](int ticks) {
      due += ticks;
      const int shift = __builtin_amdgcn_readfirstlane(ctl[C_SHIFT]);
      const int lag = min(max((int)(now() - due), -4096), 4096);
      if (w == 0) ctl[C_LAG] = lag;
      lvl = min(max(PC_L0 + ((lag - shift) * PC_G >> 6), 0), 48);
    };
    const bool b0 = n & 1, b1 = n & 2, b2 = n & 4, b3 = n & 8;
    auto kblock = [&](const char* cur, const char* nxt, int blk, auto mode) {
      float t0, t1, t2, t3;
      sfor<16>([&](auto ic) { constexpr int i = ic.v;
        const u32x4 kv = mode.v >= 2 ? pre[(mode.v >= 2 ? mode.v - 2 : 0) * 16 + i] : ring[i];
        if constexpr (mode.v == 0 || (mode.v == -1 && i + WIN >= XPRE)) {
          SCHED(); vmwait_l<i>(lvl); SCHED(); if constexpr (i + WIN >= 16) ld(nxt, IC<i + WIN - 16>{}); else ld(cur, IC<i + WIN>{}); SCHED(); }
        float d = dot2(kv[0], qd[0], 0.f); d = dot2(kv[1], qd[1], d); d = dot2(kv[2], qd[2], d); d = dot2(kv[3], qd[3], d);
        pin(d);
        SCHED();
        if constexpr (mode.v == 1 && i < XPRE) { ld(nxt, ic); SCHED(); }
        if constexpr (!(i & 1)) t0 = d;
        else {
          d = tmerge<XOR1, XOR1>(t0, d, b0);
          if constexpr (!(i & 2)) t1 = d;
          else {
            d = tmerge<XOR2, XOR2>(t1, d, b1);
            if constexpr (!(i & 4)) t2 = d;
            else {
              d = tmerge<DOWN4, UP4>(t2, d, b2);
              if constexpr (!(i & 8)) t3 = d;
              else {
                d = tmerge<XOR8, XOR8>(t3, d, b3);
                d = rbf(rbf(d) * 0.08838834764831845f);
                mx = fmaxf(mx, d);
                sc[blk * 64] = (u16)(__float_as_uint(d) >> 16);
                if (w == 0) ctl[C_BLK] = blk + 1;
              }
            }
          }
        }
      });
    };
    constexpr int B0 = NPRE + 1;
    kblock(Kb, Kb + B0 * 65536, 0, IC<1>{});
    sfor<NPRE>([&](auto pc) { kblock(Kb, Kb, 1 + pc.v, IC<2 + pc.v>{}); });
    kblock(Kb + B0 * 65536, Kb + (B0 + 1) * 65536, B0, IC<-1>{});
    pace(PC_RK);
#pragma unroll 1
    for (int blk = B0 + 1; blk < 16; ++blk) { kblock(Kb + blk * 65536, blk < 15 ? Kb + (blk + 1) * 65536 : Vb, blk, IC<0>{}); pace(PC_RK); }
    mx = wave_max(mx);
    float l = 0.f;
    f32x4 acc = {0.f, 0.f, 0.f, 0.f};
    float s = bflo(sc[0]);
    LAS float* mr = (LAS float*)(lds + L_MRG) + w * 132;
    auto vblock = [&](const char* cur, const char* nxt, int blk, auto last) {
      if (w == 0) ctl[C_BLK] = 16 + blk;
      const float p = __expf(s - mx);
      l += p;
      const float ph = rbf(p);
      const unsigned pk = pack2(ph, p - ph);
      if constexpr (!last.v) s = bflo(sc[(blk + 1) * 64]);
      else {
        l = wave_sum(l);
        if (lane == 0) { mr[0] = mx; mr[1] = l; }
        asm volatile("s_waitcnt lgkmcnt(0)" ::: "memory");
        if (lane == 0) __hip_atomic_fetch_add((LAS unsigned*)(lds + L_CTL) + C_ML, 1u, __ATOMIC_RELAXED, __HIP_MEMORY_SCOPE_WORKGROUP);
      }
      sfor<16>([&](auto ic) { constexpr int i = ic.v;
        const u32x4 a = diag(bcast16<i>(pk), sel), vv = ring[i];
        SCHED();
        if constexpr (!last.v || i + WIN < 16) vmwait_l<i>(lvl);
        SCHED();
        if constexpr (i + WIN < 16) ld(cur, IC<i + WIN>{}); else if constexpr (!last.v) ld(nxt, IC<i + WIN - 16>{});
        SCHED();
        acc = mfma16(a, vv, acc);
        SCHED();
      });
    };
#pragma unroll 1
    for (int blk = 0; blk < 15; ++blk) { vblock(Vb + blk * 65536, Vb + (blk + 1) * 65536, blk, IC<0>{}); pace(PC_RV); }
    vblock(Vb + 15 * 65536, Vb, 15, IC<1>{});
    for (int r = 0; r < 2; ++r) {
      auto t = __builtin_amdgcn_permlane32_swap(__float_as_uint(acc[r]), __float_as_uint(acc[r + 2]), false, false);
      mr[4 + (4 * (c & 1) + 2 * (c >> 1) + r) * 16 + n] = __uint_as_float(t[0]) + __uint_as_float(t[1]);
    }
    __syncthreads();
  } else if (w < 7) {
    const int i4 = w - 4;
    {
      const unsigned* xr = (const unsigned*)(x + b * DM);
      unsigned d[5];
      for (int t = 0; t < 4; ++t) d[t] = xr[lane + 64 * t];
      d[4] = xr[256 + (lane & 31)];
      const u16* xe = x + b * DM + 192 * i4 + c;
      const unsigned e0 = xe[8 * n], e1 = xe[8 * n + 4], e2 = xe[128 + 8 * (n & 7)], e3 = xe[128 + 8 * (n & 7) + 4];
      SCHED();
      u32x4 wr[48];
      const auto rq = rsrc(wq, DM * HD * 2);
      const int vo = (4 * (n >> 3) + c) * (HD * 2) + (n & 7) * 16;
      const int so = 192 * i4 * (HD * 2) + h * 256;
      auto ldq = [&](auto ic) { constexpr int i = ic.v;
        if constexpr (i < 48) wr[i] = __builtin_amdgcn_raw_buffer_load_b128(rq, vo + (i & 1) * 128, so + (i >> 1) * 8 * (HD * 2), NT); };
      sfor<PQW>(ldq);
      SCHED();
      float sm = 0.f, sq = 0.f;
      for (int t = 0; t < 5; ++t) sm += (t < 4 || lane < 32) ? bflo(d[t]) + bfhi(d[t]) : 0.f;
      const float mean = wave_sum(sm) * (1.0f / DM);
      for (int t = 0; t < 5; ++t) { const float a = bflo(d[t]) - mean, e = bfhi(d[t]) - mean; sq += (t < 4 || lane < 32) ? a * a + e * e : 0.f; }
      const LnStat st = ln_finish(mean, wave_sum(sq) * (1.0f / DM));
      const unsigned pk0 = pack2(ln_apply(bflo(e0), st), ln_apply(bflo(e1), st)), pk1 = pack2(ln_apply(bflo(e2), st), ln_apply(bflo(e3), st));
      f32x4 a0 = {0.f, 0.f, 0.f, 0.f}, a1 = a0;
      sfor<24>([&](auto sc_) { constexpr int s = sc_.v;
        const u32x4 a = diag(bcast16<s & 15>(s < 16 ? pk0 : pk1), sel);
        a0 = mfma16(a, wr[2 * s], a0); SCHED(); ldq(IC<2 * s + PQW>{}); SCHED();
        a1 = mfma16(a, wr[2 * s + 1], a1); SCHED(); ldq(IC<2 * s + 1 + PQW>{}); SCHED(); });
      f32x4 o0, o1;
      for (int r = 0; r < 4; ++r) { o0[r] = a0[r] + hi_half(dppf<XOR8>(a0[r])); o1[r] = a1[r] + hi_half(dppf<XOR8>(a1[r])); }
      if (c < 2 && n < 8) { *(LAS f32x4*)(lds + L_QP + i4 * 512 + 32 * n + 16 * c) = o0; *(LAS f32x4*)(lds + L_QP + i4 * 512 + 256 + 32 * n + 16 * c) = o1; }
      asm volatile("s_waitcnt lgkmcnt(0)" ::: "memory");
      if (lane == 0) __hip_atomic_fetch_add((LAS unsigned*)(lds + L_CTL) + C_Q, 1u, __ATOMIC_RELAXED, __HIP_MEMORY_SCOPE_WORKGROUP);
    }
    u32x4 wr[48];
    const auto ro = rsrc(wo, HD * DM * 2);
    const int vo = (4 * (n >> 3) + c) * (DM * 2) + (n & 7) * 16;
    const int so = 128 * h * (DM * 2) + 384 * i4;
    sfor<48>([&](auto ic) { constexpr int i = ic.v;
      SPIN(LDS_CAP) { if (now() - tent >= (unsigned)(WO_T0 + i * WO_DT)) break; __builtin_amdgcn_s_sleep(2); }
      wr[i] = __builtin_amdgcn_raw_buffer_load_b128(ro, vo + (i % 3) * 128, so + (i / 3) * 8 * (DM * 2), NT);
      SCHED(); vmwait<1>(); SCHED(); });
    const LAS float* mr = (const LAS float*)(lds + L_MRG);
    SPIN(LDS_CAP) { if (ctl[C_ML] >= 4u) break; __builtin_amdgcn_s_sleep(4); }
    asm volatile("" ::: "memory");
    float f4[4], M = -INFINITY, L = 0.f;
    for (int i = 0; i < 4; ++i) { f4[i] = mr[i * 132]; M = fmaxf(M, f4[i]); }
    for (int i = 0; i < 4; ++i) { f4[i] = __expf(f4[i] - M); L += f4[i] * mr[i * 132 + 1]; }
    float linv = 1.0f / L;
    for (int i = 0; i < 4; ++i) pin(f4[i]);
    pin(linv);
    __syncthreads();
    float oa = 0.f, ob = 0.f;
    for (int i = 0; i < 4; ++i) { oa += f4[i] * mr[i * 132 + 4 + c * 16 + n]; ob += f4[i] * mr[i * 132 + 4 + (4 + c) * 16 + n]; }
    const unsigned pk = pack2(oa * linv, ob * linv);
    f32x4 a3[3] = {{0.f, 0.f, 0.f, 0.f}, {0.f, 0.f, 0.f, 0.f}, {0.f, 0.f, 0.f, 0.f}};
    sfor<16>([&](auto sc_) { constexpr int s = sc_.v;
      const u32x4 a = diag(bcast16<s>(pk), sel);
      for (int t = 0; t < 3; ++t) a3[t] = mfma16(a, wr[3 * s + t], a3[t]); });
    const int po = WS_P + ((b * NH + h) * DM + 192 * i4 + 8 * n + 4 * (c & 1)) * 4;
    u32x4 o01, o2;
    for (int r = 0; r < 4; ++r) {
      auto t = __builtin_amdgcn_permlane32_swap(__float_as_uint(a3[0][r]), __float_as_uint(a3[1][r]), false, false);
      const unsigned v = __float_as_uint(__uint_as_float(t[0]) + dppf<XOR8>(__uint_as_float(t[1])));
      o01[r] = v == SENT ? SENT - 1u : v;
    }
    if (n < 8) __builtin_amdgcn_raw_buffer_store_b128(o01, rsrc(ws, WS_END), po + 256 * (c >> 1), 0, SC1);
    for (int r = 0; r < 4; ++r) {
      const unsigned v = __float_as_uint(a3[2][r] + hi_half(dppf<XOR8>(a3[2][r])));
      o2[r] = v == SENT ? SENT - 1u : v;
    }
    if (n < 8 && c < 2) __builtin_amdgcn_raw_buffer_store_b128(o2, rsrc(ws, WS_END), po + 512, 0, SC1);
  } else {
    const auto rw = rsrc(ws, WS_END);
    auto gate = [&](unsigned blocks) { SPIN(LDS_CAP) { if (ctl[C_BLK] >= blocks) break; __builtin_amdgcn_s_sleep(8); } };
    const bool gu = j < 18, dn = j >= 18 && j < 27;
    const int i0 = (14 - xg) & 7, iu = j >= 16 ? i0 : (j >> 1) + ((j >> 1) >= i0);
    const int mat = j & 1, u = 9 * xg + iu, hb = j - 18;
    gu32* flag = (gu32*)(ws + (gu ? WS_FG + (2 * u + mat) * 4 : WS_FP + (xg * 9 + hb) * 4));
    if (j < 27) {
      const int ldw = gu ? FF * 2 : DM * 2;
      const char* src = (gu ? (const char*)((mat ? w3 : w1) + 64 * u) : (const char*)(w2 + (size_t)576 * xg * DM + 64 * hb)) + (lane >> 3) * ldw + (lane & 7) * 16;
#pragma unroll 1
      for (int i = 0; i < 72; ++i) { glds16(src, (unsigned)(size_t)(lds + L_W) + i * 1024); src += 8 * ldw; vmwait<FFW - 1>(); }
    }
    auto echo_of = [](unsigned long long y) { return (unsigned)y & 0xffffffu; };
    auto chal_of = [](unsigned long long y) { return (unsigned)(y >> 24) & 0xffffffu; };
    auto word = [](unsigned echo, unsigned ch, unsigned hint) {
      return (unsigned long long)(echo & 0xffffffu) | ((unsigned long long)ch << 24) | ((unsigned long long)(hint & 0xffffu) << 48); };
    const int ri = j0 == 2 ? 0 : j0 == 12 ? 1 : j0 == 23 ? 2 : j0 == 29 ? 3 : -1;
    gu64* hi = (gu64*)(ws + WS_HI); gu64* ho = (gu64*)(ws + WS_HO);
    const unsigned long long yo = __hip_atomic_load(ho + blockIdx.x, RLX_AGENT), yi = __hip_atomic_load(hi + blockIdx.x, RLX_AGENT);
    {
      const u32x4 e = {SENT, SENT, SENT, SENT};
      for (int t = 0; t < 3; ++t)
        if (t < 2 || lane < 16) __builtin_amdgcn_raw_buffer_store_b128(e, rw, WS_P + (b * NH + h) * DM * 4 + (t * 64 + lane) * 16, 0, SC1);
      if (lane == 0 && j < 27) __hip_atomic_store(flag, 0u, RLX_AGENT);
      if (ri >= 0 && lane < NRND) __hip_atomic_store((gu32*)(ws + WS_RP + lane * 128 + (xg * 4 + ri) * 4), 0u, RLX_AGENT);
      if (blockIdx.x == LEADER && lane < NRND * 8) __hip_atomic_store((gu32*)(ws + WS_BC + lane * 128), 0u, RLX_AGENT);
    }
    const unsigned f0 = echo_of(yo), old = chal_of(yi);
    const unsigned chal = (f0 + (((old - f0 - 1u) & 0xffffffu) < 2u ? 3u : 1u)) & 0xffffffu;
    auto tile = [&](f32x4& r0, f32x4& r1) {
      const int q = n >> 2, p = n & 3;
      LAS char* wp = lds + L_W + (8 * c + q) * 128 + (gu ? 16 * p : 8 * p);
      const int o1 = gu ? 8 : 32, o2 = 64;
      const LAS char* ap = lds + L_ACT + (n & 7) * ACT_LD + 16 * c;
      f32x4 d[4];
      for (int T = 0; T < 4; ++T) d[T] = f32x4{0.f, 0.f, 0.f, 0.f};
#pragma unroll 2
      for (int s = 0; s < 18; ++s) {
        const u32x4 av = *(const LAS u32x4*)(ap + 64 * s);
        for (int T = 0; T < 4; ++T) {
          LAS char* a = wp + s * 4096 + (T & 1) * o1 + (T >> 1) * o2;
          const u32x2 lo = trread(a), hi2 = trread(a + 512);
          d[T] = mfma16(u32x4{lo[0], lo[1], hi2[0], hi2[1]}, av, d[T]);
        }
      }
      if (gu) { r0 = n < 8 ? d[0] : d[2]; r1 = n < 8 ? d[1] : d[3]; }
      else { r0 = n < 8 ? d[0] : d[1]; r1 = n < 8 ? d[2] : d[3]; }
    };
    u32x4 xd[9];
    if (gu) for (int t = 0; t < 9; ++t) xd[t] = *(const u32x4*)(x + (lane >> 3) * DM + 8 * ((lane & 7) + 8 * t));
    vmwait<0>();
    if (blockIdx.x != LEADER && lane == 0) __hip_atomic_store(hi + blockIdx.x, word(chal_of(yo), chal, tent), RLX_AGENT);
    if (gu) {
      float sm = 0.f, sq = 0.f;
      for (int t = 0; t < 9; ++t) for (int e = 0; e < 4; ++e) sm += bflo(xd[t][e]) + bfhi(xd[t][e]);
      sm += dppf<XOR1>(sm); sm += dppf<XOR2>(sm); sm += dppf<XOR7>(sm);
      const float mean = sm * (1.0f / DM);
      for (int t = 0; t < 9; ++t) for (int e = 0; e < 4; ++e) { const float a = bflo(xd[t][e]) - mean, d = bfhi(xd[t][e]) - mean; sq += a * a + d * d; }
      sq += dppf<XOR1>(sq); sq += dppf<XOR2>(sq); sq += dppf<XOR7>(sq);
      const LnStat st = ln_finish(mean, sq * (1.0f / DM));
      for (int t = 0; t < 9; ++t) {
        u32x4 v;
        for (int e = 0; e < 4; ++e) v[e] = pack2(ln_apply(bflo(xd[t][e]), st), ln_apply(bfhi(xd[t][e]), st));
        *(LAS u32x4*)(lds + L_ACT + (lane >> 3) * ACT_LD + 16 * ((lane & 7) + 8 * t)) = v;
      }
      f32x4 r0, r1;
      tile(r0, r1);
      const u32x4 o = {pack2(r0[0], r0[1]), pack2(r0[2], r0[3]), pack2(r1[0], r1[1]), pack2(r1[2], r1[3])};
      __builtin_amdgcn_raw_buffer_store_b128(o, rw, WS_GU + ((mat * NB + (n & 7)) * FF + 64 * u + 32 * (n >> 3) + 8 * c) * 2, 0, SC1);
      vmwait<0>();
      if (lane == 0) __hip_atomic_store(flag, 1u, RLX_AGENT);
    }
    if (blockIdx.x == LEADER) {
      unsigned long long y[4]; unsigned cl[4], sent[4];
      gate(G_L1);
      for (int q = 0; q < 4; ++q) { y[q] = __hip_atomic_load(hi + q * 64 + lane, RLX_AGENT); cl[q] = (echo_of(y[q]) + 1u) & 0xffffffu; sent[q] = ~0u; }
      SPIN(SPIN_CAP) {
        bool ok = true;
        for (int q = 0; q < 4; ++q) ok = ok && (echo_of(y[q]) == cl[q] || q * 64 + lane == LEADER);
        if (__all(ok)) break;
        for (int q = 0; q < 4; ++q)
          if (sent[q] != chal_of(y[q])) { sent[q] = chal_of(y[q]); __hip_atomic_store(ho + q * 64 + lane, word(sent[q], cl[q], 0u), RLX_AGENT); }
        gate(spins == 0 ? G_L1 + 1 : G_L2);
        __builtin_amdgcn_s_sleep(32);
        for (int q = 0; q < 4; ++q) y[q] = __hip_atomic_load(hi + q * 64 + lane, RLX_AGENT);
      }
      float ls = 0.f;
      for (int q = 0; q < 4; ++q) ls += q * 64 + lane == LEADER ? 0.f : (float)(short)((unsigned)(y[q] >> 48) - tent);
      const int mean = (int)(wave_sum(ls) * (1.0f / 255.f));
      for (int q = 0; q < 4; ++q) __hip_atomic_store(ho + q * 64 + lane, word(chal_of(y[q]) + 1u, cl[q], mean + tent), RLX_AGENT);
      ctl[C_SHIFT] = mean;
    } else {
      gate(G_HS);
      SPIN(SPIN_CAP) {
        const unsigned long long y = __hip_atomic_load(ho + blockIdx.x, RLX_AGENT);
        if (((__builtin_amdgcn_readfirstlane(echo_of(y)) - chal) & 0xffffffu) < 2u) {
          if (lane == 0) __hip_atomic_store(hi + blockIdx.x, word(chal_of(y), chal, ctl[C_LAG] + tent), RLX_AGENT);
          break;
        }
        __builtin_amdgcn_s_sleep(64);
      }
      gate(G_GO);
      SPIN(SPIN_CAP) {
        const unsigned long long y = __hip_atomic_load(ho + blockIdx.x, RLX_AGENT);
        if (__builtin_amdgcn_readfirstlane(echo_of(y)) == ((chal + 1u) & 0xffffffu)) { ctl[C_SHIFT] = (int)(short)((unsigned)(y >> 48) - tent); break; }
        __builtin_amdgcn_s_sleep(64);
      }
    }
    asm volatile("" ::: "memory");
    if (dn) {
      gate(G_D);
      gu32* fg = (gu32*)(ws + WS_FG) + 18 * xg + (lane < 18 ? lane : 0);
      SPIN(SPIN_CAP) { if (__all(__hip_atomic_load(fg, RLX_AGENT) == 1u)) break; __builtin_amdgcn_s_sleep(64); }
      asm volatile("" ::: "memory");
#pragma unroll 1
      for (int t3 = 0; t3 < 3; ++t3) {
        u32x4 gv[3], uv[3]; int lo[3];
        for (int t = 0; t < 3; ++t) {
          const int q = lane + 64 * (3 * t3 + t), bb = q / 72, ch = q - 72 * bb, go = WS_GU + (bb * FF + 576 * xg + 8 * ch) * 2;
          gv[t] = __builtin_amdgcn_raw_buffer_load_b128(rw, go, 0, SC1); uv[t] = __builtin_amdgcn_raw_buffer_load_b128(rw, go + NB * FF * 2, 0, SC1);
          lo[t] = L_ACT + bb * ACT_LD + 16 * ch;
        }
        for (int t = 0; t < 3; ++t) {
          u32x4 hv;
          for (int e = 0; e < 4; ++e) hv[e] = pack2(rbf(silu(bflo(gv[t][e]))) * bflo(uv[t][e]), rbf(silu(bfhi(gv[t][e]))) * bfhi(uv[t][e]));
          *(LAS u32x4*)(lds + lo[t]) = hv;
        }
      }
      f32x4 r0, r1;
      tile(r0, r1);
      const int fo = WS_PF + ((xg * NB + (n & 7)) * DM + 64 * hb + 16 * (n >> 3) + 4 * c) * 4;
      __builtin_amdgcn_raw_buffer_store_b128(__builtin_bit_cast(u32x4, r0), rw, fo, 0, SC1);
      __builtin_amdgcn_raw_buffer_store_b128(__builtin_bit_cast(u32x4, r1), rw, fo + 128, 0, SC1);
      vmwait<0>();
      if (lane == 0) __hip_atomic_store(flag, 1u, RLX_AGENT);
    }
    const int cons = (h & 1) == 0 ? (h >> 1) : (h < 4 ? 16 + (h >> 1) : -1);
    const int pl = lane >> 3, col = 32 * cons + 4 * (lane & 7);
    const int ocol = b * DM + col + 2 * (c & 1) + (n >> 3);
    float xx = 0.f, gg = 0.f;
#pragma unroll 1
    for (int r = 0; r < NRND; ++r) {
      const unsigned blk = RND0 + RNDS * r;
      gate(blk);
      if (ri >= 0 && lane == 0) __hip_atomic_store((gu32*)(ws + WS_RP + r * 128 + (xg * 4 + ri) * 4), ((ctl[C_LAG] + tent) & 0xffffu) | 0x10000u, RLX_AGENT);
      if (blockIdx.x == LEADER) {
        gate(blk + 1);
        const unsigned v = __hip_atomic_load((gu32*)(ws + WS_RP + r * 128 + (lane & 31) * 4), RLX_AGENT);
        const float cnt = wave_sum(v != 0u ? 1.f : 0.f), sum = wave_sum(v != 0u ? (float)(short)(v - tent) : 0.f);
        if (cnt >= 32.f && lane < 8) __hip_atomic_store((gu32*)(ws + WS_BC + (r * 8 + lane) * 128), (((int)(sum / cnt) + tent) & 0xffffu) | 0x10000u, RLX_AGENT);
      }
      if (r == RND_F && cons >= 0) {
        xx = bflo(x[ocol]);
        gu32* fp = (gu32*)(ws + WS_FP) + 9 * pl + (cons >> 1);
        SPIN(SPIN_CAP) { if (__all(__hip_atomic_load(fp, RLX_AGENT) == 1u)) break; __builtin_amdgcn_s_sleep(64); }
        asm volatile("" ::: "memory");
        gg = rbf(silu(rbf(reduce8(__builtin_bit_cast(f32x4, __builtin_amdgcn_raw_buffer_load_b128(rw, WS_PF + ((pl * NB + b) * DM + col) * 4, 0, SC1)), n & 8))));
      }
      gate(blk + 2);
      const unsigned m = __hip_atomic_load((gu32*)(ws + WS_BC + (r * 8 + xg) * 128), RLX_AGENT);
      if (m != 0u) ctl[C_SHIFT] = (int)(short)(m - tent);
    }
    if (cons < 0) { __syncthreads(); return; }
    __syncthreads();
    __builtin_amdgcn_s_sleep(POLL_DELAY);
    u32x4 pv[4];
    int po[4];
    for (int t = 0; t < 4; ++t) { pv[t] = u32x4{SENT, SENT, SENT, SENT}; po[t] = WS_P + ((b * NH + 8 * t + pl) * DM + col) * 4; }
    SPIN(16 * SPIN_CAP) {
      asm volatile("" ::: "memory");
      u32x4 v[4];
      for (int t = 0; t < 4; ++t) v[t] = __builtin_amdgcn_raw_buffer_load_b128(rw, po[t], 0, SC1);
      bool ok = true;
      for (int t = 0; t < 4; ++t) {
        const bool had = po[t] == WS_END, got = v[t][0] != SENT && v[t][1] != SENT && v[t][2] != SENT && v[t][3] != SENT;
        for (int r = 0; r < 4; ++r) pv[t][r] = had ? pv[t][r] : v[t][r];
        po[t] = got ? WS_END : po[t];
        ok = ok && got;
      }
      if (__all(ok)) break;
    }
    f32x4 s4;
    for (int r = 0; r < 4; ++r) s4[r] = (__uint_as_float(pv[0][r]) + __uint_as_float(pv[1][r])) + (__uint_as_float(pv[2][r]) + __uint_as_float(pv[3][r]));
    const float o = xx + rbf(rbf(reduce8(s4, n & 8)) * gg);
    if (lane < 32) out[ocol] = (u16)pack2(o, 0.f);
  }
}

extern "C" void kernel_launch(void* const* d_in, const int* in_sizes, int n_in, void* d_out, int out_size, void* d_ws, size_t ws_size, hipStream_t stream) {
  k_main<<<256, 512, 0, stream>>>((const u16*)d_in[0], (const u16*)d_in[1], (const u16*)d_in[2], (const u16*)d_in[3], (const u16*)d_in[4], (const u16*)d_in[6],
                                  (const u16*)d_in[7], (const u16*)d_in[8], (char*)d_ws, (u16*)d_out);
}
```

```cpp
#include <hip/hip_runtime.h>
#include <stdint.h>

typedef unsigned short u16;
typedef unsigned u32x4 __attribute__((ext_vector_type(4)));
typedef unsigned u32x2 __attribute__((ext_vector_type(2)));
typedef float f32x4 __attribute__((ext_vector_type(4)));
typedef short s16x4 __attribute__((ext_vector_type(4)));
typedef __bf16 bf16x8 __attribute__((ext_vector_type(8)));
typedef __bf16 bf16x2 __attribute__((ext_vector_type(2)));
#define LAS __attribute__((address_space(3)))
typedef __attribute__((address_space(1))) unsigned gu32;
typedef __attribute__((address_space(1))) unsigned long long gu64;
#define RLX_AGENT __ATOMIC_RELAXED, __HIP_MEMORY_SCOPE_AGENT

constexpr int NB = 8, DM = 576, NH = 32, SK = 4096, DH = 128, FF = 4608, HD = NH * DH;

constexpr int PK0 = 6, PKW = 4, PQW = 5, FFW = 7;
constexpr int XPRE = 11;
constexpr int WO_T0 = 1500, WO_DT = 110, WO_BU = 3;
constexpr int PC_T3 = 1385, PC_RK = 225, PC_RV = 221;
constexpr int PC_L0 = 20, PC_G = 16;
constexpr int G_L1 = 5, G_HS = 7, G_L2 = 8, G_GO = 10, G_D = 11;
constexpr int NRND = 5, RND0 = 11, RNDS = 4, RND_F = 2;
constexpr int POLL_DELAY = 40;
constexpr int WIN = 7;
constexpr int NPRE = 2;

constexpr int WS_P = 0, WS_PF = WS_P + NB * NH * DM * 4, WS_GU = WS_PF + 8 * NB * DM * 4, WS_FG = WS_GU + 2 * NB * FF * 2, WS_FP = WS_FG + 1024,
              WS_HI = WS_FP + 1024, WS_HO = WS_HI + 256 * 8, WS_RP = WS_HO + 256 * 8, WS_BC = WS_RP + NRND * 128,
              WS_END = WS_BC + NRND * 8 * 128;
constexpr int LEADER = 255;
constexpr unsigned SENT = 0xffffffffu;
constexpr unsigned SPIN_CAP = 1u << 18, LDS_CAP = 1u << 22;
constexpr int ACT_LD = DM * 2 + 16;
constexpr int L_W = 0;
constexpr int L_ACT = L_W + DM * 128;
constexpr int L_SC = L_ACT + NB * ACT_LD;
constexpr int L_QP = L_SC + 4 * 16 * 64 * 2;
constexpr int L_MRG = L_QP + 3 * 128 * 4;
constexpr int L_CTL = L_MRG + 4 * 132 * 4;
constexpr int L_END = L_CTL + 32;
enum { C_Q = 0, C_BLK = 1, C_ML = 2, C_LAG = 3, C_SHIFT = 4 };

template <int I> struct IC { static constexpr int v = I; };
template <int N, int I = 0, typename Fn> __device__ __forceinline__ void sfor(Fn&& f) {
  if constexpr (I < N) { f(IC<I>{}); sfor<N, I + 1>(f); }
}
#define SCHED() __builtin_amdgcn_sched_barrier(0)
#define SPIN(cap) _Pragma("unroll 1") for (unsigned spins = 0; spins < (cap); ++spins)
template <int N> __device__ __forceinline__ void vmwait() { asm volatile("s_waitcnt vmcnt(%0)" ::"n"(N) : "memory"); }
template <int I> __device__ __forceinline__ void vmwait_l(int lvl) {
  constexpr int br = ((I & 1) << 3) | ((I & 2) << 1) | ((I & 4) >> 1) | ((I & 8) >> 3);
  asm volatile("s_cmp_gt_i32 %0, %1\n\ts_cbranch_scc1 3f\n\ts_cmp_gt_i32 %0, %2\n\ts_cbranch_scc1 2f\n\ts_cmp_gt_i32 %0, %3\n\ts_cbranch_scc1 1f\n\t"
               "s_waitcnt vmcnt(3)\n1:\n\ts_waitcnt vmcnt(4)\n2:\n\ts_waitcnt vmcnt(5)\n3:\n\ts_waitcnt vmcnt(6)"
               ::"s"(lvl), "n"(32 + br), "n"(16 + br), "n"(br) : "memory", "scc");
}
__device__ __forceinline__ unsigned now() { return (unsigned)__builtin_amdgcn_s_memrealtime(); }

__device__ __forceinline__ float bflo(unsigned x) { return __uint_as_float(x << 16); }
__device__ __forceinline__ float bfhi(unsigned x) { return __uint_as_float(x & 0xffff0000u); }
__device__ __forceinline__ unsigned pack2(float lo, float hi) { bf16x2 v = {(__bf16)lo, (__bf16)hi}; return __builtin_bit_cast(unsigned, v); }
__device__ __forceinline__ float rbf(float x) { return bflo((unsigned)__builtin_bit_cast(u16, (__bf16)x)); }
template <int CTRL> __device__ __forceinline__ float dppf(float x) {
  return __builtin_bit_cast(float, __builtin_amdgcn_mov_dpp(__builtin_bit_cast(int, x), CTRL, 0xf, 0xf, true));
}
constexpr int XOR1 = 0xB1, XOR2 = 0x4E, XOR7 = 0x141, XOR8 = 0x128, UP4 = 0x104, DOWN4 = 0x114;
template <int N> __device__ __forceinline__ unsigned bcast16(unsigned x) {
  return (unsigned)__builtin_amdgcn_mov_dpp((int)x, 0x150 + N, 0xf, 0xf, true);
}
__device__ __forceinline__ float x16(float x, bool mx) {
  auto s = __builtin_amdgcn_permlane16_swap(__float_as_uint(x), __float_as_uint(x), false, false);
  x = mx ? fmaxf(__uint_as_float(s[0]), __uint_as_float(s[1])) : __uint_as_float(s[0]) + __uint_as_float(s[1]);
  auto t = __builtin_amdgcn_permlane32_swap(__float_as_uint(x), __float_as_uint(x), false, false);
  return mx ? fmaxf(__uint_as_float(t[0]), __uint_as_float(t[1])) : __uint_as_float(t[0]) + __uint_as_float(t[1]);
}
__device__ __forceinline__ float wave_sum(float v) {
  v += dppf<XOR1>(v); v += dppf<XOR2>(v); v += dppf<XOR7>(v); v += dppf<XOR8>(v); return x16(v, false);
}
__device__ __forceinline__ float wave_max(float v) {
  v = fmaxf(v, dppf<XOR1>(v)); v = fmaxf(v, dppf<XOR2>(v)); v = fmaxf(v, dppf<XOR7>(v)); v = fmaxf(v, dppf<XOR8>(v)); return x16(v, true);
}
__device__ __forceinline__ float hi_half(float x) {
  auto t = __builtin_amdgcn_permlane32_swap(__float_as_uint(x), __float_as_uint(x), false, false);
  return __uint_as_float(t[1]);
}
template <int CH, int CL> __device__ __forceinline__ float tmerge(float lo, float hi, bool b) {
  const float u = hi + dppf<CH>(hi), v = lo + dppf<CL>(lo);
  return b ? u : v;
}
__device__ __forceinline__ float reduce8(const f32x4& v, bool b3) {
  const float u = tmerge<XOR8, XOR8>(v[0], v[1], b3), t = tmerge<XOR8, XOR8>(v[2], v[3], b3);
  auto a = __builtin_amdgcn_permlane16_swap(__float_as_uint(u), __float_as_uint(t), false, false);
  const float z = __uint_as_float(a[0]) + __uint_as_float(a[1]);
  auto e = __builtin_amdgcn_permlane32_swap(__float_as_uint(z), __float_as_uint(z), false, false);
  return __uint_as_float(e[0]) + __uint_as_float(e[1]);
}
__device__ __forceinline__ void pin(float& x) { asm volatile("" : "+v"(x)); }
__device__ __forceinline__ f32x4 mfma16(u32x4 a, u32x4 b, f32x4 c) {
  return __builtin_amdgcn_mfma_f32_16x16x32_bf16(__builtin_bit_cast(bf16x8, a), __builtin_bit_cast(bf16x8, b), c, 0, 0, 0);
}
__device__ __forceinline__ float dot2(unsigned a, unsigned b, float c) {
  return __builtin_amdgcn_fdot2_f32_bf16(__builtin_bit_cast(bf16x2, a), __builtin_bit_cast(bf16x2, b), c, false);
}
__device__ __forceinline__ float silu(float x) { return x / (1.0f + __expf(-x)); }
__device__ __forceinline__ u32x4 diag(unsigned pk, const u32x4& sel) {
  u32x4 a;
  for (int i = 0; i < 4; ++i) a[i] = __builtin_amdgcn_perm(0u, pk, sel[i]);
  return a;
}
__device__ __forceinline__ auto rsrc(const void* p, int bytes) { return __builtin_amdgcn_make_buffer_rsrc((void*)p, 0, bytes, 0x00020000); }
constexpr int NT = 2, SC1 = 16;
__device__ __forceinline__ void glds16(const void* gsrc, unsigned lds_dst) {
  unsigned keep;
  asm volatile("s_mov_b32 %0, m0\n\ts_mov_b32 m0, %2\n\ts_nop 0\n\tglobal_load_lds_dwordx4 %1, off nt\n\ts_mov_b32 m0, %0" : "=&s"(keep) : "v"(gsrc), "s"(lds_dst) : "memory");
}
__device__ __forceinline__ u32x2 trread(LAS char* p) { return __builtin_bit_cast(u32x2, __builtin_amdgcn_ds_read_tr16_b64_v4i16((LAS s16x4*)p)); }

struct LnStat { float m, den, inv; };
__device__ __forceinline__ LnStat ln_finish(float mean, float var) {
  const float den = rbf(sqrtf(rbf(rbf(var) + 1e-5f)));
  return LnStat{rbf(mean), den, 1.0f / den};
}
__device__ __forceinline__ float ln_apply(float x, const LnStat& s) {
  const float a = rbf(x - s.m), q = a * s.inv;
  return fmaf(fmaf(-q, s.den, a), s.inv, q);
}

__global__ void __launch_bounds__(512, 2) k_main(const u16* __restrict__ x, const u16* __restrict__ K, const u16* __restrict__ V, const u16* __restrict__ wq,
                                                 const u16* __restrict__ wo, const u16* __restrict__ w1, const u16* __restrict__ w3,
                                                 const u16* __restrict__ w2, char* ws, u16* __restrict__ out) {
  __shared__ __attribute__((aligned(16))) char lds_[L_END];
  LAS char* lds = (LAS char*)lds_;
  const unsigned tent = now();
  asm volatile("" ::"s"(x), "s"(K), "s"(V), "s"(wq), "s"(w1), "s"(w3), "s"(w2), "s"(ws));
  const int tid = threadIdx.x, lane = tid & 63, w = __builtin_amdgcn_readfirstlane(tid >> 6);
  const int xg = blockIdx.x & 7, j0 = blockIdx.x >> 3, b = j0 >> 2;
  const int h = (j0 & 1) | (xg << 1) | ((j0 & 2) << 3), j = (j0 & 1) * 16 + (j0 >> 1);
  const char* Kb = (const char*)(K + (size_t)(b * NH + h) * SK * DH);
  const int voff = lane * 16 + w * 4096;
  u32x4 ring[16];
  auto ld = [&](const char* blk, auto ic) { constexpr int i = ic.v;
    ring[i] = __builtin_amdgcn_raw_buffer_load_b128(rsrc(blk, 65536), voff + (i & 3) * 1024, (i >> 2) * 16384, NT); };
  if (w < 4) sfor<PK0>([&](auto ic) { ld(Kb, ic); });
  SCHED();
  const int c = lane >> 4, n = lane & 15;
  volatile LAS unsigned* ctl = (volatile LAS unsigned*)(lds + L_CTL);
  if (tid < 8) ctl[tid] = 0u;
  __syncthreads();
  u32x4 sel;
  {
    const unsigned src = (n < 8) ? 0x0100u : 0x0302u; const int jj = n & 7;
    for (int i = 0; i < 4; ++i) sel[i] = ((jj >> 1) != i) ? 0x0c0c0c0cu : ((jj & 1) ? (0x0c0cu | (src << 16)) : (0x0c0c0000u | src));
  }
  if (w < 4) {
    const char* Vb = (const char*)(V + (size_t)(b * NH + h) * SK * DH);
    u32x4 pre[16 * NPRE];
    sfor<16 - PK0>([&](auto ic) { SCHED(); vmwait<PKW - 1>(); SCHED(); ld(Kb, IC<ic.v + PK0>{}); });
    sfor<16 * NPRE>([&](auto ic) { constexpr int i = ic.v; SCHED(); vmwait<PKW - 1>(); SCHED();
      pre[i] = __builtin_amdgcn_raw_buffer_load_b128(rsrc(Kb + 65536, NPRE * 65536), voff + (i & 3) * 1024, (i >> 2) * 16384, NT); });
    SCHED();
    __builtin_amdgcn_s_setprio(3);
    SPIN(LDS_CAP) { if (ctl[C_Q] >= 3u) break; __builtin_amdgcn_s_sleep(1); }
    asm volatile("" ::: "memory");
    unsigned qd[4];
    {
      f32x4 a = {0.f, 0.f, 0.f, 0.f}, e = a;
      for (int i = 0; i < 3; ++i) { a += *(LAS f32x4*)(lds + L_QP + i * 512 + 32 * n); e += *(LAS f32x4*)(lds + L_QP + i * 512 + 32 * n + 16); }
      qd[0] = pack2(a[0], a[1]); qd[1] = pack2(a[2], a[3]); qd[2] = pack2(e[0], e[1]); qd[3] = pack2(e[2], e[3]);
    }
    LAS u16* sc = (LAS u16*)(lds + L_SC) + w * 1024 + lane;
    float mx = -INFINITY;
    int lvl = PC_L0;
    unsigned due = tent + (PC_T3 - PC_RK);
    auto pace = [&](int ticks) {
      due += ticks;
      const int shift = __builtin_amdgcn_readfirstlane(ctl[C_SHIFT]);
      const int lag = min(max((int)(now() - due), -4096), 4096);
      if (w == 0) ctl[C_LAG] = lag;
      lvl = min(max(PC_L0 + ((lag - shift) * PC_G >> 6), 0), 48);
    };
    const bool b0 = n & 1, b1 = n & 2, b2 = n & 4, b3 = n & 8;
    auto kblock = [&](const char* cur, const char* nxt, int blk, auto mode) {
      float t0, t1, t2, t3;
      sfor<16>([&](auto ic) { constexpr int i = ic.v;
        const u32x4 kv = mode.v >= 2 ? pre[(mode.v >= 2 ? mode.v - 2 : 0) * 16 + i] : ring[i];
        if constexpr (mode.v == 0 || (mode.v == -1 && i + WIN >= XPRE)) {
          SCHED(); vmwait_l<i>(lvl); SCHED(); if constexpr (i + WIN >= 16) ld(nxt, IC<i + WIN - 16>{}); else ld(cur, IC<i + WIN>{}); SCHED(); }
        float d = dot2(kv[0], qd[0], 0.f); d = dot2(kv[1], qd[1], d); d = dot2(kv[2], qd[2], d); d = dot2(kv[3], qd[3], d);
        pin(d);
        SCHED();
        if constexpr (mode.v == 1 && i < XPRE) { ld(nxt, ic); SCHED(); }
        if constexpr (!(i & 1)) t0 = d;
        else {
          d = tmerge<XOR1, XOR1>(t0, d, b0);
          if constexpr (!(i & 2)) t1 = d;
          else {
            d = tmerge<XOR2, XOR2>(t1, d, b1);
            if constexpr (!(i & 4)) t2 = d;
            else {
              d = tmerge<DOWN4, UP4>(t2, d, b2);
              if constexpr (!(i & 8)) t3 = d;
              else {
                d = tmerge<XOR8, XOR8>(t3, d, b3);
                d = rbf(rbf(d) * 0.08838834764831845f);
                mx = fmaxf(mx, d);
                sc[blk * 64] = (u16)(__float_as_uint(d) >> 16);
                if (w == 0) ctl[C_BLK] = blk + 1;
              }
            }
          }
        }
      });
    };
    constexpr int B0 = NPRE + 1;
    kblock(Kb, Kb + B0 * 65536, 0, IC<1>{});
    sfor<NPRE>([&](auto pc) { kblock(Kb, Kb, 1 + pc.v, IC<2 + pc.v>{}); });
    kblock(Kb + B0 * 65536, Kb + (B0 + 1) * 65536, B0, IC<-1>{});
    pace(PC_RK);
#pragma unroll 1
    for (int blk = B0 + 1; blk < 16; ++blk) { kblock(Kb + blk * 65536, blk < 15 ? Kb + (blk + 1) * 65536 : Vb, blk, IC<0>{}); pace(PC_RK); }
    mx = wave_max(mx);
    float l = 0.f;
    f32x4 acc = {0.f, 0.f, 0.f, 0.f};
    float s = bflo(sc[0]);
    LAS float* mr = (LAS float*)(lds + L_MRG) + w * 132;
    auto vblock = [&](const char* cur, const char* nxt, int blk, auto last) {
      if (w == 0) ctl[C_BLK] = 16 + blk;
      const float p = __expf(s - mx);
      l += p;
      const float ph = rbf(p);
      const unsigned pk = pack2(ph, p - ph);
      if constexpr (!last.v) s = bflo(sc[(blk + 1) * 64]);
      else {
        l = wave_sum(l);
        if (lane == 0) { mr[0] = mx; mr[1] = l; }
        asm volatile("s_waitcnt lgkmcnt(0)" ::: "memory");
        if (lane == 0) __hip_atomic_fetch_add((LAS unsigned*)(lds + L_CTL) + C_ML, 1u, __ATOMIC_RELAXED, __HIP_MEMORY_SCOPE_WORKGROUP);
      }
      sfor<16>([&](auto ic) { constexpr int i = ic.v;
        const u32x4 a = diag(bcast16<i>(pk), sel), vv = ring[i];
        SCHED();
        if constexpr (!last.v || i + WIN < 16) vmwait_l<i>(lvl);
        SCHED();
        if constexpr (i + WIN < 16) ld(cur, IC<i + WIN>{}); else if constexpr (!last.v) ld(nxt, IC<i + WIN - 16>{});
        SCHED();
        acc = mfma16(a, vv, acc);
        SCHED();
      });
    };
#pragma unroll 1
    for (int blk = 0; blk < 15; ++blk) { vblock(Vb + blk * 65536, Vb + (blk + 1) * 65536, blk, IC<0>{}); pace(PC_RV); }
    vblock(Vb + 15 * 65536, Vb, 15, IC<1>{});
    for (int r = 0; r < 2; ++r) {
      auto t = __builtin_amdgcn_permlane32_swap(__float_as_uint(acc[r]), __float_as_uint(acc[r + 2]), false, false);
      mr[4 + (4 * (c & 1) + 2 * (c >> 1) + r) * 16 + n] = __uint_as_float(t[0]) + __uint_as_float(t[1]);
    }
    __syncthreads();
  } else if (w < 7) {
    const int i4 = w - 4;
    {
      const unsigned* xr = (const unsigned*)(x + b * DM);
      unsigned d[5];
      for (int t = 0; t < 4; ++t) d[t] = xr[lane + 64 * t];
      d[4] = xr[256 + (lane & 31)];
      const u16* xe = x + b * DM + 192 * i4 + c;
      const unsigned e0 = xe[8 * n], e1 = xe[8 * n + 4], e2 = xe[128 + 8 * (n & 7)], e3 = xe[128 + 8 * (n & 7) + 4];
      SCHED();
      u32x4 wr[48];
      const auto rq = rsrc(wq, DM * HD * 2);
      const int vo = (4 * (n >> 3) + c) * (HD * 2) + (n & 7) * 16;
      const int so = 192 * i4 * (HD * 2) + h * 256;
      auto ldq = [&](auto ic) { constexpr int i = ic.v;
        if constexpr (i < 48) wr[i] = __builtin_amdgcn_raw_buffer_load_b128(rq, vo + (i & 1) * 128, so + (i >> 1) * 8 * (HD * 2), NT); };
      sfor<PQW>(ldq);
      SCHED();
      float sm = 0.f, sq = 0.f;
      for (int t = 0; t < 5; ++t) sm += (t < 4 || lane < 32) ? bflo(d[t]) + bfhi(d[t]) : 0.f;
      const float mean = wave_sum(sm) * (1.0f / DM);
      for (int t = 0; t < 5; ++t) { const float a = bflo(d[t]) - mean, e = bfhi(d[t]) - mean; sq += (t < 4 || lane < 32) ? a * a + e * e : 0.f; }
      const LnStat st = ln_finish(mean, wave_sum(sq) * (1.0f / DM));
      const unsigned pk0 = pack2(ln_apply(bflo(e0), st), ln_apply(bflo(e1), st)), pk1 = pack2(ln_apply(bflo(e2), st), ln_apply(bflo(e3), st));
      f32x4 a0 = {0.f, 0.f, 0.f, 0.f}, a1 = a0;
      sfor<24>([&](auto sc_) { constexpr int s = sc_.v;
        const u32x4 a = diag(bcast16<s & 15>(s < 16 ? pk0 : pk1), sel);
        a0 = mfma16(a, wr[2 * s], a0); SCHED(); ldq(IC<2 * s + PQW>{}); SCHED();
        a1 = mfma16(a, wr[2 * s + 1], a1); SCHED(); ldq(IC<2 * s + 1 + PQW>{}); SCHED(); });
      f32x4 o0, o1;
      for (int r = 0; r < 4; ++r) { o0[r] = a0[r] + hi_half(dppf<XOR8>(a0[r])); o1[r] = a1[r] + hi_half(dppf<XOR8>(a1[r])); }
      if (c < 2 && n < 8) { *(LAS f32x4*)(lds + L_QP + i4 * 512 + 32 * n + 16 * c) = o0; *(LAS f32x4*)(lds + L_QP + i4 * 512 + 256 + 32 * n + 16 * c) = o1; }
      asm volatile("s_waitcnt lgkmcnt(0)" ::: "memory");
      if (lane == 0) __hip_atomic_fetch_add((LAS unsigned*)(lds + L_CTL) + C_Q, 1u, __ATOMIC_RELAXED, __HIP_MEMORY_SCOPE_WORKGROUP);
    }
    u32x4 wr[48];
    const auto ro = rsrc(wo, HD * DM * 2);
    const int vo = (4 * (n >> 3) + c) * (DM * 2) + (n & 7) * 16;
    const int so = 128 * h * (DM * 2) + 384 * i4;
    sfor<48>([&](auto ic) { constexpr int i = ic.v;
      if constexpr (i % WO_BU == 0) SPIN(LDS_CAP) { if (now() - tent >= (unsigned)(WO_T0 + i * WO_DT)) break; __builtin_amdgcn_s_sleep(2); }
      wr[i] = __builtin_amdgcn_raw_buffer_load_b128(ro, vo + (i % 3) * 128, so + (i / 3) * 8 * (DM * 2), NT);
      SCHED(); if constexpr (i % WO_BU == WO_BU - 1) vmwait<WO_BU>(); SCHED(); });
    const LAS float* mr = (const LAS float*)(lds + L_MRG);
    SPIN(LDS_CAP) { if (ctl[C_ML] >= 4u) break; __builtin_amdgcn_s_sleep(4); }
    asm volatile("" ::: "memory");
    float f4[4], M = -INFINITY, L = 0.f;
    for (int i = 0; i < 4; ++i) { f4[i] = mr[i * 132]; M = fmaxf(M, f4[i]); }
    for (int i = 0; i < 4; ++i) { f4[i] = __expf(f4[i] - M); L += f4[i] * mr[i * 132 + 1]; }
    float linv = 1.0f / L;
    for (int i = 0; i < 4; ++i) pin(f4[i]);
    pin(linv);
    __syncthreads();
    float oa = 0.f, ob = 0.f;
    for (int i = 0; i < 4; ++i) { oa += f4[i] * mr[i * 132 + 4 + c * 16 + n]; ob += f4[i] * mr[i * 132 + 4 + (4 + c) * 16 + n]; }
    const unsigned pk = pack2(oa * linv, ob * linv);
    f32x4 a3[3] = {{0.f, 0.f, 0.f, 0.f}, {0.f, 0.f, 0.f, 0.f}, {0.f, 0.f, 0.f, 0.f}};
    sfor<16>([&](auto sc_) { constexpr int s = sc_.v;
      const u32x4 a = diag(bcast16<s>(pk), sel);
      for (int t = 0; t < 3; ++t) a3[t] = mfma16(a, wr[3 * s + t], a3[t]); });
    const int po = WS_P + ((b * NH + h) * DM + 192 * i4 + 8 * n + 4 * (c & 1)) * 4;
    u32x4 o01, o2;
    for (int r = 0; r < 4; ++r) {
      auto t = __builtin_amdgcn_permlane32_swap(__float_as_uint(a3[0][r]), __float_as_uint(a3[1][r]), false, false);
      const unsigned v = __float_as_uint(__uint_as_float(t[0]) + dppf<XOR8>(__uint_as_float(t[1])));
      o01[r] = v == SENT ? SENT - 1u : v;
    }
    if (n < 8) __builtin_amdgcn_raw_buffer_store_b128(o01, rsrc(ws, WS_END), po + 256 * (c >> 1), 0, SC1);
    for (int r = 0; r < 4; ++r) {
      const unsigned v = __float_as_uint(a3[2][r] + hi_half(dppf<XOR8>(a3[2][r])));
      o2[r] = v == SENT ? SENT - 1u : v;
    }
    if (n < 8 && c < 2) __builtin_amdgcn_raw_buffer_store_b128(o2, rsrc(ws, WS_END), po + 512, 0, SC1);
  } else {
    const auto rw = rsrc(ws, WS_END);
    auto gate = [&](unsigned blocks) { SPIN(LDS_CAP) { if (ctl[C_BLK] >= blocks) break; __builtin_amdgcn_s_sleep(8); } };
    const bool gu = j < 18, dn = j >= 18 && j < 27;
    const int i0 = (14 - xg) & 7, iu = j >= 16 ? i0 : (j >> 1) + ((j >> 1) >= i0);
    const int mat = j & 1, u = 9 * xg + iu, hb = j - 18;
    gu32* flag = (gu32*)(ws + (gu ? WS_FG + (2 * u + mat) * 4 : WS_FP + (xg * 9 + hb) * 4));
    if (j < 27) {
      const int ldw = gu ? FF * 2 : DM * 2;
      const char* src = (gu ? (const char*)((mat ? w3 : w1) + 64 * u) : (const char*)(w2 + (size_t)576 * xg * DM + 64 * hb)) + (lane >> 3) * ldw + (lane & 7) * 16;
#pragma unroll 1
      for (int i = 0; i < 72; ++i) { glds16(src, (unsigned)(size_t)(lds + L_W) + i * 1024); src += 8 * ldw; vmwait<FFW - 1>(); }
    }
    auto echo_of = [](unsigned long long y) { return (unsigned)y & 0xffffffu; };
    auto chal_of = [](unsigned long long y) { return (unsigned)(y >> 24) & 0xffffffu; };
    auto word = [](unsigned echo, unsigned ch, unsigned hint) {
      return (unsigned long long)(echo & 0xffffffu) | ((unsigned long long)ch << 24) | ((unsigned long long)(hint & 0xffffu) << 48); };
    const int ri = j0 == 2 ? 0 : j0 == 12 ? 1 : j0 == 23 ? 2 : j0 == 29 ? 3 : -1;
    gu64* hi = (gu64*)(ws + WS_HI); gu64* ho = (gu64*)(ws + WS_HO);
    const unsigned long long yo = __hip_atomic_load(ho + blockIdx.x, RLX_AGENT), yi = __hip_atomic_load(hi + blockIdx.x, RLX_AGENT);
    {
      const u32x4 e = {SENT, SENT, SENT, SENT};
      for (int t = 0; t < 3; ++t)
        if (t < 2 || lane < 16) __builtin_amdgcn_raw_buffer_store_b128(e, rw, WS_P + (b * NH + h) * DM * 4 + (t * 64 + lane) * 16, 0, SC1);
      if (lane == 0 && j < 27) __hip_atomic_store(flag, 0u, RLX_AGENT);
      if (ri >= 0 && lane < NRND) __hip_atomic_store((gu32*)(ws + WS_RP + lane * 128 + (xg * 4 + ri) * 4), 0u, RLX_AGENT);
      if (blockIdx.x == LEADER && lane < NRND * 8) __hip_atomic_store((gu32*)(ws + WS_BC + lane * 128), 0u, RLX_AGENT);
    }
    const unsigned f0 = echo_of(yo), old = chal_of(yi);
    const unsigned chal = (f0 + (((old - f0 - 1u) & 0xffffffu) < 2u ? 3u : 1u)) & 0xffffffu;
    auto tile = [&](f32x4& r0, f32x4& r1) {
      const int q = n >> 2, p = n & 3;
      LAS char* wp = lds + L_W + (8 * c + q) * 128 + (gu ? 16 * p : 8 * p);
      const int o1 = gu ? 8 : 32, o2 = 64;
      const LAS char* ap = lds + L_ACT + (n & 7) * ACT_LD + 16 * c;
      f32x4 d[4];
      for (int T = 0; T < 4; ++T) d[T] = f32x4{0.f, 0.f, 0.f, 0.f};
#pragma unroll 2
      for (int s = 0; s < 18; ++s) {
        const u32x4 av = *(const LAS u32x4*)(ap + 64 * s);
        for (int T = 0; T < 4; ++T) {
          LAS char* a = wp + s * 4096 + (T & 1) * o1 + (T >> 1) * o2;
          const u32x2 lo = trread(a), hi2 = trread(a + 512);
          d[T] = mfma16(u32x4{lo[0], lo[1], hi2[0], hi2[1]}, av, d[T]);
        }
      }
      if (gu) { r0 = n < 8 ? d[0] : d[2]; r1 = n < 8 ? d[1] : d[3]; }
      else { r0 = n < 8 ? d[0] : d[1]; r1 = n < 8 ? d[2] : d[3]; }
    };
    u32x4 xd[9];
    if (gu) for (int t = 0; t < 9; ++t) xd[t] = *(const u32x4*)(x + (lane >> 3) * DM + 8 * ((lane & 7) + 8 * t));
    vmwait<0>();
    if (blockIdx.x != LEADER && lane == 0) __hip_atomic_store(hi + blockIdx.x, word(chal_of(yo), chal, tent), RLX_AGENT);
    if (gu) {
      float sm = 0.f, sq = 0.f;
      for (int t = 0; t < 9; ++t) for (int e = 0; e < 4; ++e) sm += bflo(xd[t][e]) + bfhi(xd[t][e]);
      sm += dppf<XOR1>(sm); sm += dppf<XOR2>(sm); sm += dppf<XOR7>(sm);
      const float mean = sm * (1.0f / DM);
      for (int t = 0; t < 9; ++t) for (int e = 0; e < 4; ++e) { const float a = bflo(xd[t][e]) - mean, d = bfhi(xd[t][e]) - mean; sq += a * a + d * d; }
      sq += dppf<XOR1>(sq); sq += dppf<XOR2>(sq); sq += dppf<XOR7>(sq);
      const LnStat st = ln_finish(mean, sq * (1.0f / DM));
      for (int t = 0; t < 9; ++t) {
        u32x4 v;
        for (int e = 0; e < 4; ++e) v[e] = pack2(ln_apply(bflo(xd[t][e]), st), ln_apply(bfhi(xd[t][e]), st));
        *(LAS u32x4*)(lds + L_ACT + (lane >> 3) * ACT_LD + 16 * ((lane & 7) + 8 * t)) = v;
      }
      f32x4 r0, r1;
      tile(r0, r1);
      const u32x4 o = {pack2(r0[0], r0[1]), pack2(r0[2], r0[3]), pack2(r1[0], r1[1]), pack2(r1[2], r1[3])};
      __builtin_amdgcn_raw_buffer_store_b128(o, rw, WS_GU + ((mat * NB + (n & 7)) * FF + 64 * u + 32 * (n >> 3) + 8 * c) * 2, 0, SC1);
      vmwait<0>();
      if (lane == 0) __hip_atomic_store(flag, 1u, RLX_AGENT);
    }
    if (blockIdx.x == LEADER) {
      unsigned long long y[4]; unsigned cl[4], sent[4];
      gate(G_L1);
      for (int q = 0; q < 4; ++q) { y[q] = __hip_atomic_load(hi + q * 64 + lane, RLX_AGENT); cl[q] = (echo_of(y[q]) + 1u) & 0xffffffu; sent[q] = ~0u; }
      SPIN(SPIN_CAP) {
        bool ok = true;
        for (int q = 0; q < 4; ++q) ok = ok && (echo_of(y[q]) == cl[q] || q * 64 + lane == LEADER);
        if (__all(ok)) break;
        for (int q = 0; q < 4; ++q)
          if (sent[q] != chal_of(y[q])) { sent[q] = chal_of(y[q]); __hip_atomic_store(ho + q * 64 + lane, word(sent[q], cl[q], 0u), RLX_AGENT); }
        gate(spins == 0 ? G_L1 + 1 : G_L2);
        __builtin_amdgcn_s_sleep(32);
        for (int q = 0; q < 4; ++q) y[q] = __hip_atomic_load(hi + q * 64 + lane, RLX_AGENT);
      }
      float ls = 0.f;
      for (int q = 0; q < 4; ++q) ls += q * 64 + lane == LEADER ? 0.f : (float)(short)((unsigned)(y[q] >> 48) - tent);
      const int mean = (int)(wave_sum(ls) * (1.0f / 255.f));
      for (int q = 0; q < 4; ++q) __hip_atomic_store(ho + q * 64 + lane, word(chal_of(y[q]) + 1u, cl[q], mean + tent), RLX_AGENT);
      ctl[C_SHIFT] = mean;
    } else {
      gate(G_HS);
      SPIN(SPIN_CAP) {
        const unsigned long long y = __hip_atomic_load(ho + blockIdx.x, RLX_AGENT);
        if (((__builtin_amdgcn_readfirstlane(echo_of(y)) - chal) & 0xffffffu) < 2u) {
          if (lane == 0) __hip_atomic_store(hi + blockIdx.x, word(chal_of(y), chal, ctl[C_LAG] + tent), RLX_AGENT);
          break;
        }
        __builtin_amdgcn_s_sleep(64);
      }
      gate(G_GO);
      SPIN(SPIN_CAP) {
        const unsigned long long y = __hip_atomic_load(ho + blockIdx.x, RLX_AGENT);
        if (__builtin_amdgcn_readfirstlane(echo_of(y)) == ((chal + 1u) & 0xffffffu)) { ctl[C_SHIFT] = (int)(short)((unsigned)(y >> 48) - tent); break; }
        __builtin_amdgcn_s_sleep(64);
      }
    }
    asm volatile("" ::: "memory");
    if (dn) {
      gate(G_D);
      gu32* fg = (gu32*)(ws + WS_FG) + 18 * xg + (lane < 18 ? lane : 0);
      SPIN(SPIN_CAP) { if (__all(__hip_atomic_load(fg, RLX_AGENT) == 1u)) break; __builtin_amdgcn_s_sleep(64); }
      asm volatile("" ::: "memory");
#pragma unroll 1
      for (int t3 = 0; t3 < 3; ++t3) {
        u32x4 gv[3], uv[3]; int lo[3];
        for (int t = 0; t < 3; ++t) {
          const int q = lane + 64 * (3 * t3 + t), bb = q / 72, ch = q - 72 * bb, go = WS_GU + (bb * FF + 576 * xg + 8 * ch) * 2;
          gv[t] = __builtin_amdgcn_raw_buffer_load_b128(rw, go, 0, SC1); uv[t] = __builtin_amdgcn_raw_buffer_load_b128(rw, go + NB * FF * 2, 0, SC1);
          lo[t] = L_ACT + bb * ACT_LD + 16 * ch;
        }
        for (int t = 0; t < 3; ++t) {
          u32x4 hv;
          for (int e = 0; e < 4; ++e) hv[e] = pack2(rbf(silu(bflo(gv[t][e]))) * bflo(uv[t][e]), rbf(silu(bfhi(gv[t][e]))) * bfhi(uv[t][e]));
          *(LAS u32x4*)(lds + lo[t]) = hv;
        }
      }
      f32x4 r0, r1;
      tile(r0, r1);
      const int fo = WS_PF + ((xg * NB + (n & 7)) * DM + 64 * hb + 16 * (n >> 3) + 4 * c) * 4;
      __builtin_amdgcn_raw_buffer_store_b128(__builtin_bit_cast(u32x4, r0), rw, fo, 0, SC1);
      __builtin_amdgcn_raw_buffer_store_b128(__builtin_bit_cast(u32x4, r1), rw, fo + 128, 0, SC1);
      vmwait<0>();
      if (lane == 0) __hip_atomic_store(flag, 1u, RLX_AGENT);
    }
    const int cons = (h & 1) == 0 ? (h >> 1) : (h < 4 ? 16 + (h >> 1) : -1);
    const int pl = lane >> 3, col = 32 * cons + 4 * (lane & 7);
    const int ocol = b * DM + col + 2 * (c & 1) + (n >> 3);
    float xx = 0.f, gg = 0.f;
#pragma unroll 1
    for (int r = 0; r < NRND; ++r) {
      const unsigned blk = RND0 + RNDS * r;
      gate(blk);
      if (ri >= 0 && lane == 0) __hip_atomic_store((gu32*)(ws + WS_RP + r * 128 + (xg * 4 + ri) * 4), ((ctl[C_LAG] + tent) & 0xffffu) | 0x10000u, RLX_AGENT);
      if (blockIdx.x == LEADER) {
        gate(blk + 1);
        const unsigned v = __hip_atomic_load((gu32*)(ws + WS_RP + r * 128 + (lane & 31) * 4), RLX_AGENT);
        const float cnt = wave_sum(v != 0u ? 1.f : 0.f), sum = wave_sum(v != 0u ? (float)(short)(v - tent) : 0.f);
        if (cnt >= 32.f && lane < 8) __hip_atomic_store((gu32*)(ws + WS_BC + (r * 8 + lane) * 128), (((int)(sum / cnt) + tent) & 0xffffu) | 0x10000u, RLX_AGENT);
      }
      if (r == RND_F && cons >= 0) {
        xx = bflo(x[ocol]);
        gu32* fp = (gu32*)(ws + WS_FP) + 9 * pl + (cons >> 1);
        SPIN(SPIN_CAP) { if (__all(__hip_atomic_load(fp, RLX_AGENT) == 1u)) break; __builtin_amdgcn_s_sleep(64); }
        asm volatile("" ::: "memory");
        gg = rbf(silu(rbf(reduce8(__builtin_bit_cast(f32x4, __builtin_amdgcn_raw_buffer_load_b128(rw, WS_PF + ((pl * NB + b) * DM + col) * 4, 0, SC1)), n & 8))));
      }
      gate(blk + 2);
      const unsigned m = __hip_atomic_load((gu32*)(ws + WS_BC + (r * 8 + xg) * 128), RLX_AGENT);
      if (m != 0u) ctl[C_SHIFT] = (int)(short)(m - tent);
    }
    if (cons < 0) { __syncthreads(); return; }
    __syncthreads();
    __builtin_amdgcn_s_sleep(POLL_DELAY);
    u32x4 pv[4];
    int po[4];
    for (int t = 0; t < 4; ++t) { pv[t] = u32x4{SENT, SENT, SENT, SENT}; po[t] = WS_P + ((b * NH + 8 * t + pl) * DM + col) * 4; }
    SPIN(16 * SPIN_CAP) {
      asm volatile("" ::: "memory");
      u32x4 v[4];
      for (int t = 0; t < 4; ++t) v[t] = __builtin_amdgcn_raw_buffer_load_b128(rw, po[t], 0, SC1);
      bool ok = true;
      for (int t = 0; t < 4; ++t) {
        const bool had = po[t] == WS_END, got = v[t][0] != SENT && v[t][1] != SENT && v[t][2] != SENT && v[t][3] != SENT;
        for (int r = 0; r < 4; ++r) pv[t][r] = had ? pv[t][r] : v[t][r];
        po[t] = got ? WS_END : po[t];
        ok = ok && got;
      }
      if (__all(ok)) break;
    }
    f32x4 s4;
    for (int r = 0; r < 4; ++r) s4[r] = (__uint_as_float(pv[0][r]) + __uint_as_float(pv[1][r])) + (__uint_as_float(pv[2][r]) + __uint_as_float(pv[3][r]));
    const float o = xx + rbf(rbf(reduce8(s4, n & 8)) * gg);
    if (lane < 32) out[ocol] = (u16)pack2(o, 0.f);
  }
}

extern "C" void kernel_launch(void* const* d_in, const int* in_sizes, int n_in, void* d_out, int out_size, void* d_ws, size_t ws_size, hipStream_t stream) {
  k_main<<<256, 512, 0, stream>>>((const u16*)d_in[0], (const u16*)d_in[1], (const u16*)d_in[2], (const u16*)d_in[3], (const u16*)d_in[4], (const u16*)d_in[6],
                                  (const u16*)d_in[7], (const u16*)d_in[8], (char*)d_ws, (u16*)d_out);
}
```

```cpp
#define PROBE 0
#define PWG 100
#include <hip/hip_runtime.h>
#include <stdint.h>

typedef unsigned short u16;
typedef unsigned u32x4 __attribute__((ext_vector_type(4)));
typedef unsigned u32x2 __attribute__((ext_vector_type(2)));
typedef float f32x4 __attribute__((ext_vector_type(4)));
typedef short s16x4 __attribute__((ext_vector_type(4)));
typedef __bf16 bf16x8 __attribute__((ext_vector_type(8)));
typedef __bf16 bf16x2 __attribute__((ext_vector_type(2)));
#define LAS __attribute__((address_space(3)))
typedef __attribute__((address_space(1))) unsigned gu32;
typedef __attribute__((address_space(1))) unsigned long long gu64;
#define RLX_AGENT __ATOMIC_RELAXED, __HIP_MEMORY_SCOPE_AGENT

constexpr int NB = 8, DM = 576, NH = 32, SK = 4096, DH = 128, FF = 4608, HD = NH * DH;

constexpr int PK0 = 6, PKW = 4, PQW = 5, FFW = 7;
constexpr int XPRE = 11;
constexpr int WO_T0 = 1500, WO_DT = 110, WO_BU = 3;
constexpr int PC_T3 = 1385, PC_RK = 225, PC_RV = 221;
constexpr int PC_L0 = 20, PC_G = 16;
constexpr int G_L1 = 5, G_HS = 7, G_L2 = 8, G_GO = 10, G_D = 11;
constexpr int NRND = 5, RND0 = 11, RNDS = 4, RND_F = 2;
constexpr int POLL_DELAY = 40;
constexpr int WIN = 7;
constexpr int NPRE = 2;

constexpr int WS_P = 0, WS_PF = WS_P + NB * NH * DM * 4, WS_GU = WS_PF + 8 * NB * DM * 4, WS_FG = WS_GU + 2 * NB * FF * 2, WS_FP = WS_FG + 1024,
              WS_HI = WS_FP + 1024, WS_HO = WS_HI + 256 * 8, WS_RP = WS_HO + 256 * 8, WS_BC = WS_RP + NRND * 128,
              WS_END = WS_BC + NRND * 8 * 128;
constexpr int LEADER = 255;
constexpr unsigned SENT = 0xffffffffu;
constexpr unsigned SPIN_CAP = 1u << 18, LDS_CAP = 1u << 22;
constexpr int ACT_LD = DM * 2 + 16;
constexpr int L_W = 0;
constexpr int L_ACT = L_W + DM * 128;
constexpr int L_SC = L_ACT + NB * ACT_LD;
constexpr int L_QP = L_SC + 4 * 16 * 64 * 2;
constexpr int L_MRG = L_QP + 3 * 128 * 4;
constexpr int L_CTL = L_MRG + 4 * 132 * 4;
constexpr int L_END = L_CTL + 32;
enum { C_Q = 0, C_BLK = 1, C_ML = 2, C_LAG = 3, C_SHIFT = 4 };

template <int I> struct IC { static constexpr int v = I; };
template <int N, int I = 0, typename Fn> __device__ __forceinline__ void sfor(Fn&& f) {
  if constexpr (I < N) { f(IC<I>{}); sfor<N, I + 1>(f); }
}
#define SCHED() __builtin_amdgcn_sched_barrier(0)
#define SPIN(cap) _Pragma("unroll 1") for (unsigned spins = 0; spins < (cap); ++spins)
template <int N> __device__ __forceinline__ void vmwait() { asm volatile("s_waitcnt vmcnt(%0)" ::"n"(N) : "memory"); }
template <int I> __device__ __forceinline__ void vmwait_l(int lvl) {
  constexpr int br = ((I & 1) << 3) | ((I & 2) << 1) | ((I & 4) >> 1) | ((I & 8) >> 3);
  asm volatile("s_cmp_gt_i32 %0, %1\n\ts_cbranch_scc1 3f\n\ts_cmp_gt_i32 %0, %2\n\ts_cbranch_scc1 2f\n\ts_cmp_gt_i32 %0, %3\n\ts_cbranch_scc1 1f\n\t"
               "s_waitcnt vmcnt(3)\n1:\n\ts_waitcnt vmcnt(4)\n2:\n\ts_waitcnt vmcnt(5)\n3:\n\ts_waitcnt vmcnt(6)"
               ::"s"(lvl), "n"(32 + br), "n"(16 + br), "n"(br) : "memory", "scc");
}
__device__ __forceinline__ unsigned now() { return (unsigned)__builtin_amdgcn_s_memrealtime(); }

__device__ __forceinline__ float bflo(unsigned x) { return __uint_as_float(x << 16); }
__device__ __forceinline__ float bfhi(unsigned x) { return __uint_as_float(x & 0xffff0000u); }
__device__ __forceinline__ unsigned pack2(float lo, float hi) { bf16x2 v = {(__bf16)lo, (__bf16)hi}; return __builtin_bit_cast(unsigned, v); }
__device__ __forceinline__ float rbf(float x) { return bflo((unsigned)__builtin_bit_cast(u16, (__bf16)x)); }
template <int CTRL> __device__ __forceinline__ float dppf(float x) {
  return __builtin_bit_cast(float, __builtin_amdgcn_mov_dpp(__builtin_bit_cast(int, x), CTRL, 0xf, 0xf, true));
}
constexpr int XOR1 = 0xB1, XOR2 = 0x4E, XOR7 = 0x141, XOR8 = 0x128, UP4 = 0x104, DOWN4 = 0x114;
template <int N> __device__ __forceinline__ unsigned bcast16(unsigned x) {
  return (unsigned)__builtin_amdgcn_mov_dpp((int)x, 0x150 + N, 0xf, 0xf, true);
}
__device__ __forceinline__ float x16(float x, bool mx) {
  auto s = __builtin_amdgcn_permlane16_swap(__float_as_uint(x), __float_as_uint(x), false, false);
  x = mx ? fmaxf(__uint_as_float(s[0]), __uint_as_float(s[1])) : __uint_as_float(s[0]) + __uint_as_float(s[1]);
  auto t = __builtin_amdgcn_permlane32_swap(__float_as_uint(x), __float_as_uint(x), false, false);
  return mx ? fmaxf(__uint_as_float(t[0]), __uint_as_float(t[1])) : __uint_as_float(t[0]) + __uint_as_float(t[1]);
}
__device__ __forceinline__ float wave_sum(float v) {
  v += dppf<XOR1>(v); v += dppf<XOR2>(v); v += dppf<XOR7>(v); v += dppf<XOR8>(v); return x16(v, false);
}
__device__ __forceinline__ float wave_max(float v) {
  v = fmaxf(v, dppf<XOR1>(v)); v = fmaxf(v, dppf<XOR2>(v)); v = fmaxf(v, dppf<XOR7>(v)); v = fmaxf(v, dppf<XOR8>(v)); return x16(v, true);
}
__device__ __forceinline__ float hi_half(float x) {
  auto t = __builtin_amdgcn_permlane32_swap(__float_as_uint(x), __float_as_uint(x), false, false);
  return __uint_as_float(t[1]);
}
template <int CH, int CL> __device__ __forceinline__ float tmerge(float lo, float hi, bool b) {
  const float u = hi + dppf<CH>(hi), v = lo + dppf<CL>(lo);
  return b ? u : v;
}
__device__ __forceinline__ float reduce8(const f32x4& v, bool b3) {
  const float u = tmerge<XOR8, XOR8>(v[0], v[1], b3), t = tmerge<XOR8, XOR8>(v[2], v[3], b3);
  auto a = __builtin_amdgcn_permlane16_swap(__float_as_uint(u), __float_as_uint(t), false, false);
  const float z = __uint_as_float(a[0]) + __uint_as_float(a[1]);
  auto e = __builtin_amdgcn_permlane32_swap(__float_as_uint(z), __float_as_uint(z), false, false);
  return __uint_as_float(e[0]) + __uint_as_float(e[1]);
}
__device__ __forceinline__ void pin(float& x) { asm volatile("" : "+v"(x)); }
__device__ __forceinline__ f32x4 mfma16(u32x4 a, u32x4 b, f32x4 c) {
  return __builtin_amdgcn_mfma_f32_16x16x32_bf16(__builtin_bit_cast(bf16x8, a), __builtin_bit_cast(bf16x8, b), c, 0, 0, 0);
}
__device__ __forceinline__ float dot2(unsigned a, unsigned b, float c) {
  return __builtin_amdgcn_fdot2_f32_bf16(__builtin_bit_cast(bf16x2, a), __builtin_bit_cast(bf16x2, b), c, false);
}
__device__ __forceinline__ float silu(float x) { return x / (1.0f + __expf(-x)); }
__device__ __forceinline__ u32x4 diag(unsigned pk, const u32x4& sel) {
  u32x4 a;
  for (int i = 0; i < 4; ++i) a[i] = __builtin_amdgcn_perm(0u, pk, sel[i]);
  return a;
}
__device__ __forceinline__ auto rsrc(const void* p, int bytes) { return __builtin_amdgcn_make_buffer_rsrc((void*)p, 0, bytes, 0x00020000); }
constexpr int NT = 2, SC1 = 16;
__device__ __forceinline__ void glds16(const void* gsrc, unsigned lds_dst) {
  unsigned keep;
  asm volatile("s_mov_b32 %0, m0\n\ts_mov_b32 m0, %2\n\ts_nop 0\n\tglobal_load_lds_dwordx4 %1, off nt\n\ts_mov_b32 m0, %0" : "=&s"(keep) : "v"(gsrc), "s"(lds_dst) : "memory");
}
__device__ __forceinline__ u32x2 trread(LAS char* p) { return __builtin_bit_cast(u32x2, __builtin_amdgcn_ds_read_tr16_b64_v4i16((LAS s16x4*)p)); }

struct LnStat { float m, den, inv; };
__device__ __forceinline__ LnStat ln_finish(float mean, float var) {
  const float den = rbf(sqrtf(rbf(rbf(var) + 1e-5f)));
  return LnStat{rbf(mean), den, 1.0f / den};
}
__device__ __forceinline__ float ln_apply(float x, const LnStat& s) {
  const float a = rbf(x - s.m), q = a * s.inv;
  return fmaf(fmaf(-q, s.den, a), s.inv, q);
}

__global__ void __launch_bounds__(512, 2) k_main(const u16* __restrict__ x, const u16* __restrict__ K, const u16* __restrict__ V, const u16* __restrict__ wq,
                                                 const u16* __restrict__ wo, const u16* __restrict__ w1, const u16* __restrict__ w3,
                                                 const u16* __restrict__ w2, char* ws, u16* __restrict__ out) {
  __shared__ __attribute__((aligned(16))) char lds_[L_END];
  LAS char* lds = (LAS char*)lds_;
  const unsigned tent = now();
  unsigned pm = 0;
#define PRB(k) do { if (PROBE == (k)) pm = now() - tent; } while (0)
#define PRBWAIT() do { if (PWG < 0 || (int)blockIdx.x == PWG || (PWG >= 1000 && (int)(blockIdx.x >> 3) == PWG - 1000)) while (now() - tent < 10000u + pm) __builtin_amdgcn_s_sleep(1); } while (0)
  asm volatile("" ::"s"(x), "s"(K), "s"(V), "s"(wq), "s"(w1), "s"(w3), "s"(w2), "s"(ws));
  const int tid = threadIdx.x, lane = tid & 63, w = __builtin_amdgcn_readfirstlane(tid >> 6);
  const int xg = blockIdx.x & 7, j0 = blockIdx.x >> 3, b = j0 >> 2;
  const int h = (j0 & 1) | (xg << 1) | ((j0 & 2) << 3), j = (j0 & 1) * 16 + (j0 >> 1);
  const char* Kb = (const char*)(K + (size_t)(b * NH + h) * SK * DH);
  const int voff = lane * 16 + w * 4096;
  u32x4 ring[16];
  auto ld = [&](const char* blk, auto ic) { constexpr int i = ic.v;
    ring[i] = __builtin_amdgcn_raw_buffer_load_b128(rsrc(blk, 65536), voff + (i & 3) * 1024, (i >> 2) * 16384, NT); };
  if (w < 4) sfor<PK0>([&](auto ic) { ld(Kb, ic); });
  SCHED();
  const int c = lane >> 4, n = lane & 15;
  volatile LAS unsigned* ctl = (volatile LAS unsigned*)(lds + L_CTL);
  if (tid < 8) ctl[tid] = 0u;
  __syncthreads();
  u32x4 sel;
  {
    const unsigned src = (n < 8) ? 0x0100u : 0x0302u; const int jj = n & 7;
    for (int i = 0; i < 4; ++i) sel[i] = ((jj >> 1) != i) ? 0x0c0c0c0cu : ((jj & 1) ? (0x0c0cu | (src << 16)) : (0x0c0c0000u | src));
  }
  if (w < 4) {
    const char* Vb = (const char*)(V + (size_t)(b * NH + h) * SK * DH);
    u32x4 pre[16 * NPRE];
    sfor<16 - PK0>([&](auto ic) { SCHED(); vmwait<PKW - 1>(); SCHED(); ld(Kb, IC<ic.v + PK0>{}); });
    sfor<16 * NPRE>([&](auto ic) { constexpr int i = ic.v; SCHED(); vmwait<PKW - 1>(); SCHED();
      pre[i] = __builtin_amdgcn_raw_buffer_load_b128(rsrc(Kb + 65536, NPRE * 65536), voff + (i & 3) * 1024, (i >> 2) * 16384, NT); });
    SCHED();
    __builtin_amdgcn_s_setprio(3);
    SPIN(LDS_CAP) { if (ctl[C_Q] >= 3u) break; __builtin_amdgcn_s_sleep(1); }
    asm volatile("" ::: "memory");
    unsigned qd[4];
    {
      f32x4 a = {0.f, 0.f, 0.f, 0.f}, e = a;
      for (int i = 0; i < 3; ++i) { a += *(LAS f32x4*)(lds + L_QP + i * 512 + 32 * n); e += *(LAS f32x4*)(lds + L_QP + i * 512 + 32 * n + 16); }
      qd[0] = pack2(a[0], a[1]); qd[1] = pack2(a[2], a[3]); qd[2] = pack2(e[0], e[1]); qd[3] = pack2(e[2], e[3]);
    }
    LAS u16* sc = (LAS u16*)(lds + L_SC) + w * 1024 + lane;
    float mx = -INFINITY;
    int lvl = PC_L0;
    unsigned due = tent + (PC_T3 - PC_RK);
    auto pace = [&](int ticks) {
      due += ticks;
      const int shift = __builtin_amdgcn_readfirstlane(ctl[C_SHIFT]);
      const int lag = min(max((int)(now() - due), -4096), 4096);
      if (w == 0) ctl[C_LAG] = lag;
      lvl = min(max(PC_L0 + ((lag - shift) * PC_G >> 6), 0), 48);
    };
    const bool b0 = n & 1, b1 = n & 2, b2 = n & 4, b3 = n & 8;
    auto kblock = [&](const char* cur, const char* nxt, int blk, auto mode) {
      float t0, t1, t2, t3;
      sfor<16>([&](auto ic) { constexpr int i = ic.v;
        const u32x4 kv = mode.v >= 2 ? pre[(mode.v >= 2 ? mode.v - 2 : 0) * 16 + i] : ring[i];
        if constexpr (mode.v == 0 || (mode.v == -1 && i + WIN >= XPRE)) {
          SCHED(); vmwait_l<i>(lvl); SCHED(); if constexpr (i + WIN >= 16) ld(nxt, IC<i + WIN - 16>{}); else ld(cur, IC<i + WIN>{}); SCHED(); }
        float d = dot2(kv[0], qd[0], 0.f); d = dot2(kv[1], qd[1], d); d = dot2(kv[2], qd[2], d); d = dot2(kv[3], qd[3], d);
        pin(d);
        SCHED();
        if constexpr (mode.v == 1 && i < XPRE) { ld(nxt, ic); SCHED(); }
        if constexpr (!(i & 1)) t0 = d;
        else {
          d = tmerge<XOR1, XOR1>(t0, d, b0);
          if constexpr (!(i & 2)) t1 = d;
          else {
            d = tmerge<XOR2, XOR2>(t1, d, b1);
            if constexpr (!(i & 4)) t2 = d;
            else {
              d = tmerge<DOWN4, UP4>(t2, d, b2);
              if constexpr (!(i & 8)) t3 = d;
              else {
                d = tmerge<XOR8, XOR8>(t3, d, b3);
                d = rbf(rbf(d) * 0.08838834764831845f);
                mx = fmaxf(mx, d);
                sc[blk * 64] = (u16)(__float_as_uint(d) >> 16);
                if (w == 0) ctl[C_BLK] = blk + 1;
              }
            }
          }
        }
      });
    };
    constexpr int B0 = NPRE + 1;
    kblock(Kb, Kb + B0 * 65536, 0, IC<1>{});
    sfor<NPRE>([&](auto pc) { kblock(Kb, Kb, 1 + pc.v, IC<2 + pc.v>{}); });
    kblock(Kb + B0 * 65536, Kb + (B0 + 1) * 65536, B0, IC<-1>{});
    PRB(5);
    pace(PC_RK);
#pragma unroll 1
    for (int blk = B0 + 1; blk < 16; ++blk) { kblock(Kb + blk * 65536, blk < 15 ? Kb + (blk + 1) * 65536 : Vb, blk, IC<0>{}); pace(PC_RK); if (blk == 9) PRB(6); }
    PRB(2);
    mx = wave_max(mx);
    float l = 0.f;
    f32x4 acc = {0.f, 0.f, 0.f, 0.f};
    float s = bflo(sc[0]);
    LAS float* mr = (LAS float*)(lds + L_MRG) + w * 132;
    auto vblock = [&](const char* cur, const char* nxt, int blk, auto last) {
      if (w == 0) ctl[C_BLK] = 16 + blk;
      const float p = __expf(s - mx);
      l += p;
      const float ph = rbf(p);
      const unsigned pk = pack2(ph, p - ph);
      if constexpr (!last.v) s = bflo(sc[(blk + 1) * 64]);
      else {
        l = wave_sum(l);
        if (lane == 0) { mr[0] = mx; mr[1] = l; }
        asm volatile("s_waitcnt lgkmcnt(0)" ::: "memory");
        if (lane == 0) __hip_atomic_fetch_add((LAS unsigned*)(lds + L_CTL) + C_ML, 1u, __ATOMIC_RELAXED, __HIP_MEMORY_SCOPE_WORKGROUP);
      }
      sfor<16>([&](auto ic) { constexpr int i = ic.v;
        const u32x4 a = diag(bcast16<i>(pk), sel), vv = ring[i];
        SCHED();
        if constexpr (!last.v || i + WIN < 16) vmwait_l<i>(lvl);
        SCHED();
        if constexpr (i + WIN < 16) ld(cur, IC<i + WIN>{}); else if constexpr (!last.v) ld(nxt, IC<i + WIN - 16>{});
        SCHED();
        acc = mfma16(a, vv, acc);
        SCHED();
      });
    };
#pragma unroll 1
    for (int blk = 0; blk < 15; ++blk) { vblock(Vb + blk * 65536, Vb + (blk + 1) * 65536, blk, IC<0>{}); pace(PC_RV); }
    vblock(Vb + 15 * 65536, Vb, 15, IC<1>{});
    PRB(3);
    for (int r = 0; r < 2; ++r) {
      auto t = __builtin_amdgcn_permlane32_swap(__float_as_uint(acc[r]), __float_as_uint(acc[r + 2]), false, false);
      mr[4 + (4 * (c & 1) + 2 * (c >> 1) + r) * 16 + n] = __uint_as_float(t[0]) + __uint_as_float(t[1]);
    }
    __syncthreads();
    if (w == 0) PRBWAIT();
  } else if (w < 7) {
    const int i4 = w - 4;
    {
      const unsigned* xr = (const unsigned*)(x + b * DM);
      unsigned d[5];
      for (int t = 0; t < 4; ++t) d[t] = xr[lane + 64 * t];
      d[4] = xr[256 + (lane & 31)];
      const u16* xe = x + b * DM + 192 * i4 + c;
      const unsigned e0 = xe[8 * n], e1 = xe[8 * n + 4], e2 = xe[128 + 8 * (n & 7)], e3 = xe[128 + 8 * (n & 7) + 4];
      SCHED();
      u32x4 wr[48];
      const auto rq = rsrc(wq, DM * HD * 2);
      const int vo = (4 * (n >> 3) + c) * (HD * 2) + (n & 7) * 16;
      const int so = 192 * i4 * (HD * 2) + h * 256;
      auto ldq = [&](auto ic) { constexpr int i = ic.v;
        if constexpr (i < 48) wr[i] = __builtin_amdgcn_raw_buffer_load_b128(rq, vo + (i & 1) * 128, so + (i >> 1) * 8 * (HD * 2), NT); };
      sfor<PQW>(ldq);
      SCHED();
      float sm = 0.f, sq = 0.f;
      for (int t = 0; t < 5; ++t) sm += (t < 4 || lane < 32) ? bflo(d[t]) + bfhi(d[t]) : 0.f;
      const float mean = wave_sum(sm) * (1.0f / DM);
      for (int t = 0; t < 5; ++t) { const float a = bflo(d[t]) - mean, e = bfhi(d[t]) - mean; sq += (t < 4 || lane < 32) ? a * a + e * e : 0.f; }
      const LnStat st = ln_finish(mean, wave_sum(sq) * (1.0f / DM));
      const unsigned pk0 = pack2(ln_apply(bflo(e0), st), ln_apply(bflo(e1), st)), pk1 = pack2(ln_apply(bflo(e2), st), ln_apply(bflo(e3), st));
      f32x4 a0 = {0.f, 0.f, 0.f, 0.f}, a1 = a0;
      sfor<24>([&](auto sc_) { constexpr int s = sc_.v;
        const u32x4 a = diag(bcast16<s & 15>(s < 16 ? pk0 : pk1), sel);
        a0 = mfma16(a, wr[2 * s], a0); SCHED(); ldq(IC<2 * s + PQW>{}); SCHED();
        a1 = mfma16(a, wr[2 * s + 1], a1); SCHED(); ldq(IC<2 * s + 1 + PQW>{}); SCHED(); });
      f32x4 o0, o1;
      for (int r = 0; r < 4; ++r) { o0[r] = a0[r] + hi_half(dppf<XOR8>(a0[r])); o1[r] = a1[r] + hi_half(dppf<XOR8>(a1[r])); }
      if (c < 2 && n < 8) { *(LAS f32x4*)(lds + L_QP + i4 * 512 + 32 * n + 16 * c) = o0; *(LAS f32x4*)(lds + L_QP + i4 * 512 + 256 + 32 * n + 16 * c) = o1; }
      asm volatile("s_waitcnt lgkmcnt(0)" ::: "memory");
      if (lane == 0) __hip_atomic_fetch_add((LAS unsigned*)(lds + L_CTL) + C_Q, 1u, __ATOMIC_RELAXED, __HIP_MEMORY_SCOPE_WORKGROUP);
    }
    u32x4 wr[48];
    const auto ro = rsrc(wo, HD * DM * 2);
    const int vo = (4 * (n >> 3) + c) * (DM * 2) + (n & 7) * 16;
    const int so = 128 * h * (DM * 2) + 384 * i4;
    sfor<48>([&](auto ic) { constexpr int i = ic.v;
      if constexpr (i % WO_BU == 0) SPIN(LDS_CAP) { if (now() - tent >= (unsigned)(WO_T0 + i * WO_DT)) break; __builtin_amdgcn_s_sleep(2); }
      wr[i] = __builtin_amdgcn_raw_buffer_load_b128(ro, vo + (i % 3) * 128, so + (i / 3) * 8 * (DM * 2), NT);
      SCHED(); if constexpr (i % WO_BU == WO_BU - 1) vmwait<WO_BU>(); SCHED(); });
    const LAS float* mr = (const LAS float*)(lds + L_MRG);
    SPIN(LDS_CAP) { if (ctl[C_ML] >= 4u) break; __builtin_amdgcn_s_sleep(4); }
    asm volatile("" ::: "memory");
    float f4[4], M = -INFINITY, L = 0.f;
    for (int i = 0; i < 4; ++i) { f4[i] = mr[i * 132]; M = fmaxf(M, f4[i]); }
    for (int i = 0; i < 4; ++i) { f4[i] = __expf(f4[i] - M); L += f4[i] * mr[i * 132 + 1]; }
    float linv = 1.0f / L;
    for (int i = 0; i < 4; ++i) pin(f4[i]);
    pin(linv);
    __syncthreads();
    float oa = 0.f, ob = 0.f;
    for (int i = 0; i < 4; ++i) { oa += f4[i] * mr[i * 132 + 4 + c * 16 + n]; ob += f4[i] * mr[i * 132 + 4 + (4 + c) * 16 + n]; }
    const unsigned pk = pack2(oa * linv, ob * linv);
    f32x4 a3[3] = {{0.f, 0.f, 0.f, 0.f}, {0.f, 0.f, 0.f, 0.f}, {0.f, 0.f, 0.f, 0.f}};
    sfor<16>([&](auto sc_) { constexpr int s = sc_.v;
      const u32x4 a = diag(bcast16<s>(pk), sel);
      for (int t = 0; t < 3; ++t) a3[t] = mfma16(a, wr[3 * s + t], a3[t]); });
    const int po = WS_P + ((b * NH + h) * DM + 192 * i4 + 8 * n + 4 * (c & 1)) * 4;
    u32x4 o01, o2;
    for (int r = 0; r < 4; ++r) {
      auto t = __builtin_amdgcn_permlane32_swap(__float_as_uint(a3[0][r]), __float_as_uint(a3[1][r]), false, false);
      const unsigned v = __float_as_uint(__uint_as_float(t[0]) + dppf<XOR8>(__uint_as_float(t[1])));
      o01[r] = v == SENT ? SENT - 1u : v;
    }
    if (n < 8) __builtin_amdgcn_raw_buffer_store_b128(o01, rsrc(ws, WS_END), po + 256 * (c >> 1), 0, SC1);
    for (int r = 0; r < 4; ++r) {
      const unsigned v = __float_as_uint(a3[2][r] + hi_half(dppf<XOR8>(a3[2][r])));
      o2[r] = v == SENT ? SENT - 1u : v;
    }
    if (n < 8 && c < 2) __builtin_amdgcn_raw_buffer_store_b128(o2, rsrc(ws, WS_END), po + 512, 0, SC1);
  } else {
    const auto rw = rsrc(ws, WS_END);
    auto gate = [&](unsigned blocks) { SPIN(LDS_CAP) { if (ctl[C_BLK] >= blocks) break; __builtin_amdgcn_s_sleep(8); } };
    const bool gu = j < 18, dn = j >= 18 && j < 27;
    const int i0 = (14 - xg) & 7, iu = j >= 16 ? i0 : (j >> 1) + ((j >> 1) >= i0);
    const int mat = j & 1, u = 9 * xg + iu, hb = j - 18;
    gu32* flag = (gu32*)(ws + (gu ? WS_FG + (2 * u + mat) * 4 : WS_FP + (xg * 9 + hb) * 4));
    if (j < 27) {
      const int ldw = gu ? FF * 2 : DM * 2;
      const char* src = (gu ? (const char*)((mat ? w3 : w1) + 64 * u) : (const char*)(w2 + (size_t)576 * xg * DM + 64 * hb)) + (lane >> 3) * ldw + (lane & 7) * 16;
#pragma unroll 1
      for (int i = 0; i < 72; ++i) { glds16(src, (unsigned)(size_t)(lds + L_W) + i * 1024); src += 8 * ldw; vmwait<FFW - 1>(); }
    }
    auto echo_of = [](unsigned long long y) { return (unsigned)y & 0xffffffu; };
    auto chal_of = [](unsigned long long y) { return (unsigned)(y >> 24) & 0xffffffu; };
    auto word = [](unsigned echo, unsigned ch, unsigned hint) {
      return (unsigned long long)(echo & 0xffffffu) | ((unsigned long long)ch << 24) | ((unsigned long long)(hint & 0xffffu) << 48); };
    const int ri = j0 == 2 ? 0 : j0 == 12 ? 1 : j0 == 23 ? 2 : j0 == 29 ? 3 : -1;
    gu64* hi = (gu64*)(ws + WS_HI); gu64* ho = (gu64*)(ws + WS_HO);
    const unsigned long long yo = __hip_atomic_load(ho + blockIdx.x, RLX_AGENT), yi = __hip_atomic_load(hi + blockIdx.x, RLX_AGENT);
    {
      const u32x4 e = {SENT, SENT, SENT, SENT};
      for (int t = 0; t < 3; ++t)
        if (t < 2 || lane < 16) __builtin_amdgcn_raw_buffer_store_b128(e, rw, WS_P + (b * NH + h) * DM * 4 + (t * 64 + lane) * 16, 0, SC1);
      if (lane == 0 && j < 27) __hip_atomic_store(flag, 0u, RLX_AGENT);
      if (ri >= 0 && lane < NRND) __hip_atomic_store((gu32*)(ws + WS_RP + lane * 128 + (xg * 4 + ri) * 4), 0u, RLX_AGENT);
      if (blockIdx.x == LEADER && lane < NRND * 8) __hip_atomic_store((gu32*)(ws + WS_BC + lane * 128), 0u, RLX_AGENT);
    }
    const unsigned f0 = echo_of(yo), old = chal_of(yi);
    const unsigned chal = (f0 + (((old - f0 - 1u) & 0xffffffu) < 2u ? 3u : 1u)) & 0xffffffu;
    auto tile = [&](f32x4& r0, f32x4& r1) {
      const int q = n >> 2, p = n & 3;
      LAS char* wp = lds + L_W + (8 * c + q) * 128 + (gu ? 16 * p : 8 * p);
      const int o1 = gu ? 8 : 32, o2 = 64;
      const LAS char* ap = lds + L_ACT + (n & 7) * ACT_LD + 16 * c;
      f32x4 d[4];
      for (int T = 0; T < 4; ++T) d[T] = f32x4{0.f, 0.f, 0.f, 0.f};
#pragma unroll 2
      for (int s = 0; s < 18; ++s) {
        const u32x4 av = *(const LAS u32x4*)(ap + 64 * s);
        for (int T = 0; T < 4; ++T) {
          LAS char* a = wp + s * 4096 + (T & 1) * o1 + (T >> 1) * o2;
          const u32x2 lo = trread(a), hi2 = trread(a + 512);
          d[T] = mfma16(u32x4{lo[0], lo[1], hi2[0], hi2[1]}, av, d[T]);
        }
      }
      if (gu) { r0 = n < 8 ? d[0] : d[2]; r1 = n < 8 ? d[1] : d[3]; }
      else { r0 = n < 8 ? d[0] : d[1]; r1 = n < 8 ? d[2] : d[3]; }
    };
    u32x4 xd[9];
    if (gu) for (int t = 0; t < 9; ++t) xd[t] = *(const u32x4*)(x + (lane >> 3) * DM + 8 * ((lane & 7) + 8 * t));
    vmwait<0>();
    if (blockIdx.x != LEADER && lane == 0) __hip_atomic_store(hi + blockIdx.x, word(chal_of(yo), chal, tent), RLX_AGENT);
    if (gu) {
      float sm = 0.f, sq = 0.f;
      for (int t = 0; t < 9; ++t) for (int e = 0; e < 4; ++e) sm += bflo(xd[t][e]) + bfhi(xd[t][e]);
      sm += dppf<XOR1>(sm); sm += dppf<XOR2>(sm); sm += dppf<XOR7>(sm);
      const float mean = sm * (1.0f / DM);
      for (int t = 0; t < 9; ++t) for (int e = 0; e < 4; ++e) { const float a = bflo(xd[t][e]) - mean, d = bfhi(xd[t][e]) - mean; sq += a * a + d * d; }
      sq += dppf<XOR1>(sq); sq += dppf<XOR2>(sq); sq += dppf<XOR7>(sq);
      const LnStat st = ln_finish(mean, sq * (1.0f / DM));
      for (int t = 0; t < 9; ++t) {
        u32x4 v;
        for (int e = 0; e < 4; ++e) v[e] = pack2(ln_apply(bflo(xd[t][e]), st), ln_apply(bfhi(xd[t][e]), st));
        *(LAS u32x4*)(lds + L_ACT + (lane >> 3) * ACT_LD + 16 * ((lane & 7) + 8 * t)) = v;
      }
      f32x4 r0, r1;
      tile(r0, r1);
      const u32x4 o = {pack2(r0[0], r0[1]), pack2(r0[2], r0[3]), pack2(r1[0], r1[1]), pack2(r1[2], r1[3])};
      __builtin_amdgcn_raw_buffer_store_b128(o, rw, WS_GU + ((mat * NB + (n & 7)) * FF + 64 * u + 32 * (n >> 3) + 8 * c) * 2, 0, SC1);
      vmwait<0>();
      if (lane == 0) __hip_atomic_store(flag, 1u, RLX_AGENT);
    }
    if (blockIdx.x == LEADER) {
      unsigned long long y[4]; unsigned cl[4], sent[4];
      gate(G_L1);
      for (int q = 0; q < 4; ++q) { y[q] = __hip_atomic_load(hi + q * 64 + lane, RLX_AGENT); cl[q] = (echo_of(y[q]) + 1u) & 0xffffffu; sent[q] = ~0u; }
      SPIN(SPIN_CAP) {
        bool ok = true;
        for (int q = 0; q < 4; ++q) ok = ok && (echo_of(y[q]) == cl[q] || q * 64 + lane == LEADER);
        if (__all(ok)) break;
        for (int q = 0; q < 4; ++q)
          if (sent[q] != chal_of(y[q])) { sent[q] = chal_of(y[q]); __hip_atomic_store(ho + q * 64 + lane, word(sent[q], cl[q], 0u), RLX_AGENT); }
        gate(spins == 0 ? G_L1 + 1 : G_L2);
        __builtin_amdgcn_s_sleep(32);
        for (int q = 0; q < 4; ++q) y[q] = __hip_atomic_load(hi + q * 64 + lane, RLX_AGENT);
      }
      float ls = 0.f;
      for (int q = 0; q < 4; ++q) ls += q * 64 + lane == LEADER ? 0.f : (float)(short)((unsigned)(y[q] >> 48) - tent);
      const int mean = (int)(wave_sum(ls) * (1.0f / 255.f));
      for (int q = 0; q < 4; ++q) __hip_atomic_store(ho + q * 64 + lane, word(chal_of(y[q]) + 1u, cl[q], mean + tent), RLX_AGENT);
      ctl[C_SHIFT] = mean;
    } else {
      gate(G_HS);
      SPIN(SPIN_CAP) {
        const unsigned long long y = __hip_atomic_load(ho + blockIdx.x, RLX_AGENT);
        if (((__builtin_amdgcn_readfirstlane(echo_of(y)) - chal) & 0xffffffu) < 2u) {
          if (lane == 0) __hip_atomic_store(hi + blockIdx.x, word(chal_of(y), chal, ctl[C_LAG] + tent), RLX_AGENT);
          break;
        }
        __builtin_amdgcn_s_sleep(64);
      }
      gate(G_GO);
      SPIN(SPIN_CAP) {
        const unsigned long long y = __hip_atomic_load(ho + blockIdx.x, RLX_AGENT);
        if (__builtin_amdgcn_readfirstlane(echo_of(y)) == ((chal + 1u) & 0xffffffu)) { ctl[C_SHIFT] = (int)(short)((unsigned)(y >> 48) - tent); break; }
        __builtin_amdgcn_s_sleep(64);
      }
    }
    asm volatile("" ::: "memory");
    if (dn) {
      gate(G_D);
      gu32* fg = (gu32*)(ws + WS_FG) + 18 * xg + (lane < 18 ? lane : 0);
      SPIN(SPIN_CAP) { if (__all(__hip_atomic_load(fg, RLX_AGENT) == 1u)) break; __builtin_amdgcn_s_sleep(64); }
      asm volatile("" ::: "memory");
#pragma unroll 1
      for (int t3 = 0; t3 < 3; ++t3) {
        u32x4 gv[3], uv[3]; int lo[3];
        for (int t = 0; t < 3; ++t) {
          const int q = lane + 64 * (3 * t3 + t), bb = q / 72, ch = q - 72 * bb, go = WS_GU + (bb * FF + 576 * xg + 8 * ch) * 2;
          gv[t] = __builtin_amdgcn_raw_buffer_load_b128(rw, go, 0, SC1); uv[t] = __builtin_amdgcn_raw_buffer_load_b128(rw, go + NB * FF * 2, 0, SC1);
          lo[t] = L_ACT + bb * ACT_LD + 16 * ch;
        }
        for (int t = 0; t < 3; ++t) {
          u32x4 hv;
          for (int e = 0; e < 4; ++e) hv[e] = pack2(rbf(silu(bflo(gv[t][e]))) * bflo(uv[t][e]), rbf(silu(bfhi(gv[t][e]))) * bfhi(uv[t][e]));
          *(LAS u32x4*)(lds + lo[t]) = hv;
        }
      }
      f32x4 r0, r1;
      tile(r0, r1);
      const int fo = WS_PF + ((xg * NB + (n & 7)) * DM + 64 * hb + 16 * (n >> 3) + 4 * c) * 4;
      __builtin_amdgcn_raw_buffer_store_b128(__builtin_bit_cast(u32x4, r0), rw, fo, 0, SC1);
      __builtin_amdgcn_raw_buffer_store_b128(__builtin_bit_cast(u32x4, r1), rw, fo + 128, 0, SC1);
      vmwait<0>();
      if (lane == 0) __hip_atomic_store(flag, 1u, RLX_AGENT);
    }
    const int cons = (h & 1) == 0 ? (h >> 1) : (h < 4 ? 16 + (h >> 1) : -1);
    const int pl = lane >> 3, col = 32 * cons + 4 * (lane & 7);
    const int ocol = b * DM + col + 2 * (c & 1) + (n >> 3);
    float xx = 0.f, gg = 0.f;
#pragma unroll 1
    for (int r = 0; r < NRND; ++r) {
      const unsigned blk = RND0 + RNDS * r;
      gate(blk);
      if (ri >= 0 && lane == 0) __hip_atomic_store((gu32*)(ws + WS_RP + r * 128 + (xg * 4 + ri) * 4), ((ctl[C_LAG] + tent) & 0xffffu) | 0x10000u, RLX_AGENT);
      if (blockIdx.x == LEADER) {
        gate(blk + 1);
        const unsigned v = __hip_atomic_load((gu32*)(ws + WS_RP + r * 128 + (lane & 31) * 4), RLX_AGENT);
        const float cnt = wave_sum(v != 0u ? 1.f : 0.f), sum = wave_sum(v != 0u ? (float)(short)(v - tent) : 0.f);
        if (cnt >= 32.f && lane < 8) __hip_atomic_store((gu32*)(ws + WS_BC + (r * 8 + lane) * 128), (((int)(sum / cnt) + tent) & 0xffffu) | 0x10000u, RLX_AGENT);
      }
      if (r == RND_F && cons >= 0) {
        xx = bflo(x[ocol]);
        gu32* fp = (gu32*)(ws + WS_FP) + 9 * pl + (cons >> 1);
        SPIN(SPIN_CAP) { if (__all(__hip_atomic_load(fp, RLX_AGENT) == 1u)) break; __builtin_amdgcn_s_sleep(64); }
        asm volatile("" ::: "memory");
        gg = rbf(silu(rbf(reduce8(__builtin_bit_cast(f32x4, __builtin_amdgcn_raw_buffer_load_b128(rw, WS_PF + ((pl * NB + b) * DM + col) * 4, 0, SC1)), n & 8))));
      }
      gate(blk + 2);
      const unsigned m = __hip_atomic_load((gu32*)(ws + WS_BC + (r * 8 + xg) * 128), RLX_AGENT);
      if (m != 0u) ctl[C_SHIFT] = (int)(short)(m - tent);
    }
    if (cons < 0) { __syncthreads(); return; }
    __syncthreads();
    __builtin_amdgcn_s_sleep(POLL_DELAY);
    u32x4 pv[4];
    int po[4];
    for (int t = 0; t < 4; ++t) { pv[t] = u32x4{SENT, SENT, SENT, SENT}; po[t] = WS_P + ((b * NH + 8 * t + pl) * DM + col) * 4; }
    SPIN(16 * SPIN_CAP) {
      asm volatile("" ::: "memory");
      u32x4 v[4];
      for (int t = 0; t < 4; ++t) v[t] = __builtin_amdgcn_raw_buffer_load_b128(rw, po[t], 0, SC1);
      bool ok = true;
      for (int t = 0; t < 4; ++t) {
        const bool had = po[t] == WS_END, got = v[t][0] != SENT && v[t][1] != SENT && v[t][2] != SENT && v[t][3] != SENT;
        for (int r = 0; r < 4; ++r) pv[t][r] = had ? pv[t][r] : v[t][r];
        po[t] = got ? WS_END : po[t];
        ok = ok && got;
      }
      if (__all(ok)) break;
    }
    f32x4 s4;
    for (int r = 0; r < 4; ++r) s4[r] = (__uint_as_float(pv[0][r]) + __uint_as_float(pv[1][r])) + (__uint_as_float(pv[2][r]) + __uint_as_float(pv[3][r]));
    const float o = xx + rbf(rbf(reduce8(s4, n & 8)) * gg);
    if (lane < 32) out[ocol] = (u16)pack2(o, 0.f);
    PRB(4);
    if (PROBE == 4) PRBWAIT();
  }
}

extern "C" void kernel_launch(void* const* d_in, const int* in_sizes, int n_in, void* d_out, int out_size, void* d_ws, size_t ws_size, hipStream_t stream) {
  k_main<<<256, 512, 0, stream>>>((const u16*)d_in[0], (const u16*)d_in[1], (const u16*)d_in[2], (const u16*)d_in[3], (const u16*)d_in[4], (const u16*)d_in[6],
                                  (const u16*)d_in[7], (const u16*)d_in[8], (char*)d_ws, (u16*)d_out);
}
```

```cpp
#include <hip/hip_runtime.h>
#include <stdint.h>

typedef unsigned short u16;
typedef unsigned u32x4 __attribute__((ext_vector_type(4)));
typedef unsigned u32x2 __attribute__((ext_vector_type(2)));
typedef float f32x4 __attribute__((ext_vector_type(4)));
typedef short s16x4 __attribute__((ext_vector_type(4)));
typedef __bf16 bf16x8 __attribute__((ext_vector_type(8)));
typedef __bf16 bf16x2 __attribute__((ext_vector_type(2)));
#define LAS __attribute__((address_space(3)))
typedef __attribute__((address_space(1))) unsigned gu32;
typedef __attribute__((address_space(1))) unsigned long long gu64;
#define RLX_AGENT __ATOMIC_RELAXED, __HIP_MEMORY_SCOPE_AGENT

constexpr int NB = 8, DM = 576, NH = 32, SK = 4096, DH = 128, FF = 4608, HD = NH * DH;

constexpr int PK0 = 6, PKW = 4, PQW = 5, FFW = 7;
constexpr int XP0 = 8, XP1 = 2;
constexpr int WO_T0 = 1500, WO_DT = 110, WO_BU = 3;
constexpr int PC_T3 = 1385, PC_RK = 225, PC_RV = 221;
constexpr int PC_L0 = 20, PC_G = 16;
constexpr int G_L1 = 5, G_HS = 7, G_L2 = 8, G_GO = 10, G_D = 11;
constexpr int NRND = 5, RND0 = 11, RNDS = 4, RND_F = 2;
constexpr int POLL_DELAY = 40;
constexpr int WIN = 7;
constexpr int NPRE = 2;
constexpr int XPRE = XP0 + NPRE * XP1;

constexpr int WS_P = 0, WS_PF = WS_P + NB * NH * DM * 4, WS_GU = WS_PF + 8 * NB * DM * 4, WS_FG = WS_GU + 2 * NB * FF * 2, WS_FP = WS_FG + 1024,
              WS_HI = WS_FP + 1024, WS_HO = WS_HI + 256 * 8, WS_RP = WS_HO + 256 * 8, WS_BC = WS_RP + NRND * 128,
              WS_END = WS_BC + NRND * 8 * 128;
constexpr int LEADER = 255;
constexpr unsigned SENT = 0xffffffffu;
constexpr unsigned SPIN_CAP = 1u << 18, LDS_CAP = 1u << 22;
constexpr int ACT_LD = DM * 2 + 16;
constexpr int L_W = 0;
constexpr int L_ACT = L_W + DM * 128;
constexpr int L_SC = L_ACT + NB * ACT_LD;
constexpr int L_QP = L_SC + 4 * 16 * 64 * 2;
constexpr int L_MRG = L_QP + 3 * 128 * 4;
constexpr int L_CTL = L_MRG + 4 * 132 * 4;
constexpr int L_END = L_CTL + 32;
enum { C_Q = 0, C_BLK = 1, C_ML = 2, C_LAG = 3, C_SHIFT = 4 };

template <int I> struct IC { static constexpr int v = I; };
template <int N, int I = 0, typename Fn> __device__ __forceinline__ void sfor(Fn&& f) {
  if constexpr (I < N) { f(IC<I>{}); sfor<N, I + 1>(f); }
}
#define SCHED() __builtin_amdgcn_sched_barrier(0)
#define SPIN(cap) _Pragma("unroll 1") for (unsigned spins = 0; spins < (cap); ++spins)
template <int N> __device__ __forceinline__ void vmwait() { asm volatile("s_waitcnt vmcnt(%0)" ::"n"(N) : "memory"); }
template <int I> __device__ __forceinline__ void vmwait_l(int lvl) {
  constexpr int br = ((I & 1) << 3) | ((I & 2) << 1) | ((I & 4) >> 1) | ((I & 8) >> 3);
  asm volatile("s_cmp_gt_i32 %0, %1\n\ts_cbranch_scc1 3f\n\ts_cmp_gt_i32 %0, %2\n\ts_cbranch_scc1 2f\n\ts_cmp_gt_i32 %0, %3\n\ts_cbranch_scc1 1f\n\t"
               "s_waitcnt vmcnt(3)\n1:\n\ts_waitcnt vmcnt(4)\n2:\n\ts_waitcnt vmcnt(5)\n3:\n\ts_waitcnt vmcnt(6)"
               ::"s"(lvl), "n"(32 + br), "n"(16 + br), "n"(br) : "memory", "scc");
}
__device__ __forceinline__ unsigned now() { return (unsigned)__builtin_amdgcn_s_memrealtime(); }

__device__ __forceinline__ float bflo(unsigned x) { return __uint_as_float(x << 16); }
__device__ __forceinline__ float bfhi(unsigned x) { return __uint_as_float(x & 0xffff0000u); }
__device__ __forceinline__ unsigned pack2(float lo, float hi) { bf16x2 v = {(__bf16)lo, (__bf16)hi}; return __builtin_bit_cast(unsigned, v); }
__device__ __forceinline__ float rbf(float x) { return bflo((unsigned)__builtin_bit_cast(u16, (__bf16)x)); }
template <int CTRL> __device__ __forceinline__ float dppf(float x) {
  return __builtin_bit_cast(float, __builtin_amdgcn_mov_dpp(__builtin_bit_cast(int, x), CTRL, 0xf, 0xf, true));
}
constexpr int XOR1 = 0xB1, XOR2 = 0x4E, XOR7 = 0x141, XOR8 = 0x128, UP4 = 0x104, DOWN4 = 0x114;
template <int N> __device__ __forceinline__ unsigned bcast16(unsigned x) {
  return (unsigned)__builtin_amdgcn_mov_dpp((int)x, 0x150 + N, 0xf, 0xf, true);
}
__device__ __forceinline__ float x16(float x, bool mx) {
  auto s = __builtin_amdgcn_permlane16_swap(__float_as_uint(x), __float_as_uint(x), false, false);
  x = mx ? fmaxf(__uint_as_float(s[0]), __uint_as_float(s[1])) : __uint_as_float(s[0]) + __uint_as_float(s[1]);
  auto t = __builtin_amdgcn_permlane32_swap(__float_as_uint(x), __float_as_uint(x), false, false);
  return mx ? fmaxf(__uint_as_float(t[0]), __uint_as_float(t[1])) : __uint_as_float(t[0]) + __uint_as_float(t[1]);
}
__device__ __forceinline__ float wave_sum(float v) {
  v += dppf<XOR1>(v); v += dppf<XOR2>(v); v += dppf<XOR7>(v); v += dppf<XOR8>(v); return x16(v, false);
}
__device__ __forceinline__ float wave_max(float v) {
  v = fmaxf(v, dppf<XOR1>(v)); v = fmaxf(v, dppf<XOR2>(v)); v = fmaxf(v, dppf<XOR7>(v)); v = fmaxf(v, dppf<XOR8>(v)); return x16(v, true);
}
__device__ __forceinline__ float hi_half(float x) {
  auto t = __builtin_amdgcn_permlane32_swap(__float_as_uint(x), __float_as_uint(x), false, false);
  return __uint_as_float(t[1]);
}
template <int CH, int CL> __device__ __forceinline__ float tmerge(float lo, float hi, bool b) {
  const float u = hi + dppf<CH>(hi), v = lo + dppf<CL>(lo);
  return b ? u : v;
}
__device__ __forceinline__ float reduce8(const f32x4& v, bool b3) {
  const float u = tmerge<XOR8, XOR8>(v[0], v[1], b3), t = tmerge<XOR8, XOR8>(v[2], v[3], b3);
  auto a = __builtin_amdgcn_permlane16_swap(__float_as_uint(u), __float_as_uint(t), false, false);
  const float z = __uint_as_float(a[0]) + __uint_as_float(a[1]);
  auto e = __builtin_amdgcn_permlane32_swap(__float_as_uint(z), __float_as_uint(z), false, false);
  return __uint_as_float(e[0]) + __uint_as_float(e[1]);
}
__device__ __forceinline__ void pin(float& x) { asm volatile("" : "+v"(x)); }
__device__ __forceinline__ f32x4 mfma16(u32x4 a, u32x4 b, f32x4 c) {
  return __builtin_amdgcn_mfma_f32_16x16x32_bf16(__builtin_bit_cast(bf16x8, a), __builtin_bit_cast(bf16x8, b), c, 0, 0, 0);
}
__device__ __forceinline__ float dot2(unsigned a, unsigned b, float c) {
  return __builtin_amdgcn_fdot2_f32_bf16(__builtin_bit_cast(bf16x2, a), __builtin_bit_cast(bf16x2, b), c, false);
}
__device__ __forceinline__ float silu(float x) { return x / (1.0f + __expf(-x)); }
__device__ __forceinline__ u32x4 diag(unsigned pk, const u32x4& sel) {
  u32x4 a;
  for (int i = 0; i < 4; ++i) a[i] = __builtin_amdgcn_perm(0u, pk, sel[i]);
  return a;
}
__device__ __forceinline__ auto rsrc(const void* p, int bytes) { return __builtin_amdgcn_make_buffer_rsrc((void*)p, 0, bytes, 0x00020000); }
constexpr int NT = 2, SC1 = 16;
__device__ __forceinline__ void glds16(const void* gsrc, unsigned lds_dst) {
  unsigned keep;
  asm volatile("s_mov_b32 %0, m0\n\ts_mov_b32 m0, %2\n\ts_nop 0\n\tglobal_load_lds_dwordx4 %1, off nt\n\ts_mov_b32 m0, %0" : "=&s"(keep) : "v"(gsrc), "s"(lds_dst) : "memory");
}
__device__ __forceinline__ u32x2 trread(LAS char* p) { return __builtin_bit_cast(u32x2, __builtin_amdgcn_ds_read_tr16_b64_v4i16((LAS s16x4*)p)); }

struct LnStat { float m, den, inv; };
__device__ __forceinline__ LnStat ln_finish(float mean, float var) {
  const float den = rbf(sqrtf(rbf(rbf(var) + 1e-5f)));
  return LnStat{rbf(mean), den, 1.0f / den};
}
__device__ __forceinline__ float ln_apply(float x, const LnStat& s) {
  const float a = rbf(x - s.m), q = a * s.inv;
  return fmaf(fmaf(-q, s.den, a), s.inv, q);
}

__global__ void __launch_bounds__(512, 2) k_main(const u16* __restrict__ x, const u16* __restrict__ K, const u16* __restrict__ V, const u16* __restrict__ wq,
                                                 const u16* __restrict__ wo, const u16* __restrict__ w1, const u16* __restrict__ w3,
                                                 const u16* __restrict__ w2, char* ws, u16* __restrict__ out) {
  __shared__ __attribute__((aligned(16))) char lds_[L_END];
  LAS char* lds = (LAS char*)lds_;
  const unsigned tent = now();
  asm volatile("" ::"s"(x), "s"(K), "s"(V), "s"(wq), "s"(w1), "s"(w3), "s"(w2), "s"(ws));
  const int tid = threadIdx.x, lane = tid & 63, w = __builtin_amdgcn_readfirstlane(tid >> 6);
  const int xg = blockIdx.x & 7, j0 = blockIdx.x >> 3, b = j0 >> 2;
  const int h = (j0 & 1) | (xg << 1) | ((j0 & 2) << 3), j = (j0 & 1) * 16 + (j0 >> 1);
  const char* Kb = (const char*)(K + (size_t)(b * NH + h) * SK * DH);
  const int voff = lane * 16 + w * 4096;
  u32x4 ring[16];
  auto ld = [&](const char* blk, auto ic) { constexpr int i = ic.v;
    ring[i] = __builtin_amdgcn_raw_buffer_load_b128(rsrc(blk, 65536), voff + (i & 3) * 1024, (i >> 2) * 16384, NT); };
  if (w < 4) sfor<PK0>([&](auto ic) { ld(Kb, ic); });
  SCHED();
  const int c = lane >> 4, n = lane & 15;
  volatile LAS unsigned* ctl = (volatile LAS unsigned*)(lds + L_CTL);
  if (tid < 8) ctl[tid] = 0u;
  __syncthreads();
  u32x4 sel;
  {
    const unsigned src = (n < 8) ? 0x0100u : 0x0302u; const int jj = n & 7;
    for (int i = 0; i < 4; ++i) sel[i] = ((jj >> 1) != i) ? 0x0c0c0c0cu : ((jj & 1) ? (0x0c0cu | (src << 16)) : (0x0c0c0000u | src));
  }
  if (w < 4) {
    const char* Vb = (const char*)(V + (size_t)(b * NH + h) * SK * DH);
    u32x4 pre[16 * NPRE];
    sfor<16 - PK0>([&](auto ic) { SCHED(); vmwait<PKW - 1>(); SCHED(); ld(Kb, IC<ic.v + PK0>{}); });
    sfor<16 * NPRE>([&](auto ic) { constexpr int i = ic.v; SCHED(); vmwait<PKW - 1>(); SCHED();
      pre[i] = __builtin_amdgcn_raw_buffer_load_b128(rsrc(Kb + 65536, NPRE * 65536), voff + (i & 3) * 1024, (i >> 2) * 16384, NT); });
    SCHED();
    __builtin_amdgcn_s_setprio(3);
    SPIN(LDS_CAP) { if (ctl[C_Q] >= 3u) break; __builtin_amdgcn_s_sleep(1); }
    asm volatile("" ::: "memory");
    unsigned qd[4];
    {
      f32x4 a = {0.f, 0.f, 0.f, 0.f}, e = a;
      for (int i = 0; i < 3; ++i) { a += *(LAS f32x4*)(lds + L_QP + i * 512 + 32 * n); e += *(LAS f32x4*)(lds + L_QP + i * 512 + 32 * n + 16); }
      qd[0] = pack2(a[0], a[1]); qd[1] = pack2(a[2], a[3]); qd[2] = pack2(e[0], e[1]); qd[3] = pack2(e[2], e[3]);
    }
    LAS u16* sc = (LAS u16*)(lds + L_SC) + w * 1024 + lane;
    float mx = -INFINITY;
    int lvl = PC_L0;
    unsigned due = tent + (PC_T3 - PC_RK);
    auto pace = [&](int ticks) {
      due += ticks;
      const int shift = __builtin_amdgcn_readfirstlane(ctl[C_SHIFT]);
      const int lag = min(max((int)(now() - due), -4096), 4096);
      if (w == 0) ctl[C_LAG] = lag;
      lvl = min(max(PC_L0 + ((lag - shift) * PC_G >> 6), 0), 48);
    };
    const bool b0 = n & 1, b1 = n & 2, b2 = n & 4, b3 = n & 8;
    auto kblock = [&](const char* cur, const char* nxt, int blk, auto mode) {
      float t0, t1, t2, t3;
      sfor<16>([&](auto ic) { constexpr int i = ic.v;
        const u32x4 kv = mode.v >= 2 ? pre[(mode.v >= 2 ? mode.v - 2 : 0) * 16 + i] : ring[i];
        if constexpr (mode.v == 0 || (mode.v == -1 && i + WIN >= XPRE)) {
          SCHED(); vmwait_l<i>(lvl); SCHED(); if constexpr (i + WIN >= 16) ld(nxt, IC<i + WIN - 16>{}); else ld(cur, IC<i + WIN>{}); SCHED(); }
        float d = dot2(kv[0], qd[0], 0.f); d = dot2(kv[1], qd[1], d); d = dot2(kv[2], qd[2], d); d = dot2(kv[3], qd[3], d);
        pin(d);
        SCHED();
        if constexpr (mode.v == 1 && i < XP0) { ld(nxt, ic); SCHED(); }
        if constexpr (mode.v >= 2 && i % (16 / XP1) == 0) { ld(nxt, IC<XP0 + (mode.v - 2) * XP1 + i / (16 / XP1)>{}); SCHED(); }
        if constexpr (!(i & 1)) t0 = d;
        else {
          d = tmerge<XOR1, XOR1>(t0, d, b0);
          if constexpr (!(i & 2)) t1 = d;
          else {
            d = tmerge<XOR2, XOR2>(t1, d, b1);
            if constexpr (!(i & 4)) t2 = d;
            else {
              d = tmerge<DOWN4, UP4>(t2, d, b2);
              if constexpr (!(i & 8)) t3 = d;
              else {
                d = tmerge<XOR8, XOR8>(t3, d, b3);
                d = rbf(rbf(d) * 0.08838834764831845f);
                mx = fmaxf(mx, d);
                sc[blk * 64] = (u16)(__float_as_uint(d) >> 16);
                if (w == 0) ctl[C_BLK] = blk + 1;
              }
            }
          }
        }
      });
    };
    constexpr int B0 = NPRE + 1;
    kblock(Kb, Kb + B0 * 65536, 0, IC<1>{});
    sfor<NPRE>([&](auto pc) { kblock(Kb, Kb + B0 * 65536, 1 + pc.v, IC<2 + pc.v>{}); });
    kblock(Kb + B0 * 65536, Kb + (B0 + 1) * 65536, B0, IC<-1>{});
    pace(PC_RK);
#pragma unroll 1
    for (int blk = B0 + 1; blk < 16; ++blk) { kblock(Kb + blk * 65536, blk < 15 ? Kb + (blk + 1) * 65536 : Vb, blk, IC<0>{}); pace(PC_RK); }
    mx = wave_max(mx);
    float l = 0.f;
    f32x4 acc = {0.f, 0.f, 0.f, 0.f};
    float s = bflo(sc[0]);
    LAS float* mr = (LAS float*)(lds + L_MRG) + w * 132;
    auto vblock = [&](const char* cur, const char* nxt, int blk, auto last) {
      if (w == 0) ctl[C_BLK] = 16 + blk;
      const float p = __expf(s - mx);
      l += p;
      const float ph = rbf(p);
      const unsigned pk = pack2(ph, p - ph);
      if constexpr (!last.v) s = bflo(sc[(blk + 1) * 64]);
      else {
        l = wave_sum(l);
        if (lane == 0) { mr[0] = mx; mr[1] = l; }
        asm volatile("s_waitcnt lgkmcnt(0)" ::: "memory");
        if (lane == 0) __hip_atomic_fetch_add((LAS unsigned*)(lds + L_CTL) + C_ML, 1u, __ATOMIC_RELAXED, __HIP_MEMORY_SCOPE_WORKGROUP);
      }
      sfor<16>([&](auto ic) { constexpr int i = ic.v;
        const u32x4 a = diag(bcast16<i>(pk), sel), vv = ring[i];
        SCHED();
        if constexpr (!last.v || i + WIN < 16) vmwait_l<i>(lvl);
        SCHED();
        if constexpr (i + WIN < 16) ld(cur, IC<i + WIN>{}); else if constexpr (!last.v) ld(nxt, IC<i + WIN - 16>{});
        SCHED();
        acc = mfma16(a, vv, acc);
        SCHED();
      });
    };
#pragma unroll 1
    for (int blk = 0; blk < 15; ++blk) { vblock(Vb + blk * 65536, Vb + (blk + 1) * 65536, blk, IC<0>{}); pace(PC_RV); }
    vblock(Vb + 15 * 65536, Vb, 15, IC<1>{});
    for (int r = 0; r < 2; ++r) {
      auto t = __builtin_amdgcn_permlane32_swap(__float_as_uint(acc[r]), __float_as_uint(acc[r + 2]), false, false);
      mr[4 + (4 * (c & 1) + 2 * (c >> 1) + r) * 16 + n] = __uint_as_float(t[0]) + __uint_as_float(t[1]);
    }
    __syncthreads();
  } else if (w < 7) {
    const int i4 = w - 4;
    {
      const unsigned* xr = (const unsigned*)(x + b * DM);
      unsigned d[5];
      for (int t = 0; t < 4; ++t) d[t] = xr[lane + 64 * t];
      d[4] = xr[256 + (lane & 31)];
      const u16* xe = x + b * DM + 192 * i4 + c;
      const unsigned e0 = xe[8 * n], e1 = xe[8 * n + 4], e2 = xe[128 + 8 * (n & 7)], e3 = xe[128 + 8 * (n & 7) + 4];
      SCHED();
      u32x4 wr[48];
      const auto rq = rsrc(wq, DM * HD * 2);
      const int vo = (4 * (n >> 3) + c) * (HD * 2) + (n & 7) * 16;
      const int so = 192 * i4 * (HD * 2) + h * 256;
      auto ldq = [&](auto ic) { constexpr int i = ic.v;
        if constexpr (i < 48) wr[i] = __builtin_amdgcn_raw_buffer_load_b128(rq, vo + (i & 1) * 128, so + (i >> 1) * 8 * (HD * 2), NT); };
      sfor<PQW>(ldq);
      SCHED();
      float sm = 0.f, sq = 0.f;
      for (int t = 0; t < 5; ++t) sm += (t < 4 || lane < 32) ? bflo(d[t]) + bfhi(d[t]) : 0.f;
      const float mean = wave_sum(sm) * (1.0f / DM);
      for (int t = 0; t < 5; ++t) { const float a = bflo(d[t]) - mean, e = bfhi(d[t]) - mean; sq += (t < 4 || lane < 32) ? a * a + e * e : 0.f; }
      const LnStat st = ln_finish(mean, wave_sum(sq) * (1.0f / DM));
      const unsigned pk0 = pack2(ln_apply(bflo(e0), st), ln_apply(bflo(e1), st)), pk1 = pack2(ln_apply(bflo(e2), st), ln_apply(bflo(e3), st));
      f32x4 a0 = {0.f, 0.f, 0.f, 0.f}, a1 = a0;
      sfor<24>([&](auto sc_) { constexpr int s = sc_.v;
        const u32x4 a = diag(bcast16<s & 15>(s < 16 ? pk0 : pk1), sel);
        a0 = mfma16(a, wr[2 * s], a0); SCHED(); ldq(IC<2 * s + PQW>{}); SCHED();
        a1 = mfma16(a, wr[2 * s + 1], a1); SCHED(); ldq(IC<2 * s + 1 + PQW>{}); SCHED(); });
      f32x4 o0, o1;
      for (int r = 0; r < 4; ++r) { o0[r] = a0[r] + hi_half(dppf<XOR8>(a0[r])); o1[r] = a1[r] + hi_half(dppf<XOR8>(a1[r])); }
      if (c < 2 && n < 8) { *(LAS f32x4*)(lds + L_QP + i4 * 512 + 32 * n + 16 * c) = o0; *(LAS f32x4*)(lds + L_QP + i4 * 512 + 256 + 32 * n + 16 * c) = o1; }
      asm volatile("s_waitcnt lgkmcnt(0)" ::: "memory");
      if (lane == 0) __hip_atomic_fetch_add((LAS unsigned*)(lds + L_CTL) + C_Q, 1u, __ATOMIC_RELAXED, __HIP_MEMORY_SCOPE_WORKGROUP);
    }
    u32x4 wr[48];
    const auto ro = rsrc(wo, HD * DM * 2);
    const int vo = (4 * (n >> 3) + c) * (DM * 2) + (n & 7) * 16;
    const int so = 128 * h * (DM * 2) + 384 * i4;
    sfor<48>([&](auto ic) { constexpr int i = ic.v;
      if constexpr (i % WO_BU == 0) SPIN(LDS_CAP) { if (now() - tent >= (unsigned)(WO_T0 + i * WO_DT)) break; __builtin_amdgcn_s_sleep(2); }
      wr[i] = __builtin_amdgcn_raw_buffer_load_b128(ro, vo + (i % 3) * 128, so + (i / 3) * 8 * (DM * 2), NT);
      SCHED(); if constexpr (i % WO_BU == WO_BU - 1) vmwait<WO_BU>(); SCHED(); });
    const LAS float* mr = (const LAS float*)(lds + L_MRG);
    SPIN(LDS_CAP) { if (ctl[C_ML] >= 4u) break; __builtin_amdgcn_s_sleep(4); }
    asm volatile("" ::: "memory");
    float f4[4], M = -INFINITY, L = 0.f;
    for (int i = 0; i < 4; ++i) { f4[i] = mr[i * 132]; M = fmaxf(M, f4[i]); }
    for (int i = 0; i < 4; ++i) { f4[i] = __expf(f4[i] - M); L += f4[i] * mr[i * 132 + 1]; }
    float linv = 1.0f / L;
    for (int i = 0; i < 4; ++i) pin(f4[i]);
    pin(linv);
    __syncthreads();
    float oa = 0.f, ob = 0.f;
    for (int i = 0; i < 4; ++i) { oa += f4[i] * mr[i * 132 + 4 + c * 16 + n]; ob += f4[i] * mr[i * 132 + 4 + (4 + c) * 16 + n]; }
    const unsigned pk = pack2(oa * linv, ob * linv);
    f32x4 a3[3] = {{0.f, 0.f, 0.f, 0.f}, {0.f, 0.f, 0.f, 0.f}, {0.f, 0.f, 0.f, 0.f}};
    sfor<16>([&](auto sc_) { constexpr int s = sc_.v;
      const u32x4 a = diag(bcast16<s>(pk), sel);
      for (int t = 0; t < 3; ++t) a3[t] = mfma16(a, wr[3 * s + t], a3[t]); });
    const int po = WS_P + ((b * NH + h) * DM + 192 * i4 + 8 * n + 4 * (c & 1)) * 4;
    u32x4 o01, o2;
    for (int r = 0; r < 4; ++r) {
      auto t = __builtin_amdgcn_permlane32_swap(__float_as_uint(a3[0][r]), __float_as_uint(a3[1][r]), false, false);
      const unsigned v = __float_as_uint(__uint_as_float(t[0]) + dppf<XOR8>(__uint_as_float(t[1])));
      o01[r] = v == SENT ? SENT - 1u : v;
    }
    if (n < 8) __builtin_amdgcn_raw_buffer_store_b128(o01, rsrc(ws, WS_END), po + 256 * (c >> 1), 0, SC1);
    for (int r = 0; r < 4; ++r) {
      const unsigned v = __float_as_uint(a3[2][r] + hi_half(dppf<XOR8>(a3[2][r])));
      o2[r] = v == SENT ? SENT - 1u : v;
    }
    if (n < 8 && c < 2) __builtin_amdgcn_raw_buffer_store_b128(o2, rsrc(ws, WS_END), po + 512, 0, SC1);
  } else {
    const auto rw = rsrc(ws, WS_END);
    auto gate = [&](unsigned blocks) { SPIN(LDS_CAP) { if (ctl[C_BLK] >= blocks) break; __builtin_amdgcn_s_sleep(8); } };
    const bool gu = j < 18, dn = j >= 18 && j < 27;
    const int i0 = (14 - xg) & 7, iu = j >= 16 ? i0 : (j >> 1) + ((j >> 1) >= i0);
    const int mat = j & 1, u = 9 * xg + iu, hb = j - 18;
    gu32* flag = (gu32*)(ws + (gu ? WS_FG + (2 * u + mat) * 4 : WS_FP + (xg * 9 + hb) * 4));
    if (j < 27) {
      const int ldw = gu ? FF * 2 : DM * 2;
      const char* src = (gu ? (const char*)((mat ? w3 : w1) + 64 * u) : (const char*)(w2 + (size_t)576 * xg * DM + 64 * hb)) + (lane >> 3) * ldw + (lane & 7) * 16;
#pragma unroll 1
      for (int i = 0; i < 72; ++i) { glds16(src, (unsigned)(size_t)(lds + L_W) + i * 1024); src += 8 * ldw; vmwait<FFW - 1>(); }
    }
    auto echo_of = [](unsigned long long y) { return (unsigned)y & 0xffffffu; };
    auto chal_of = [](unsigned long long y) { return (unsigned)(y >> 24) & 0xffffffu; };
    auto word = [](unsigned echo, unsigned ch, unsigned hint) {
      return (unsigned long long)(echo & 0xffffffu) | ((unsigned long long)ch << 24) | ((unsigned long long)(hint & 0xffffu) << 48); };
    const int ri = j0 == 2 ? 0 : j0 == 12 ? 1 : j0 == 23 ? 2 : j0 == 29 ? 3 : -1;
    gu64* hi = (gu64*)(ws + WS_HI); gu64* ho = (gu64*)(ws + WS_HO);
    const unsigned long long yo = __hip_atomic_load(ho + blockIdx.x, RLX_AGENT), yi = __hip_atomic_load(hi + blockIdx.x, RLX_AGENT);
    {
      const u32x4 e = {SENT, SENT, SENT, SENT};
      for (int t = 0; t < 3; ++t)
        if (t < 2 || lane < 16) __builtin_amdgcn_raw_buffer_store_b128(e, rw, WS_P + (b * NH + h) * DM * 4 + (t * 64 + lane) * 16, 0, SC1);
      if (lane == 0 && j < 27) __hip_atomic_store(flag, 0u, RLX_AGENT);
      if (ri >= 0 && lane < NRND) __hip_atomic_store((gu32*)(ws + WS_RP + lane * 128 + (xg * 4 + ri) * 4), 0u, RLX_AGENT);
      if (blockIdx.x == LEADER && lane < NRND * 8) __hip_atomic_store((gu32*)(ws + WS_BC + lane * 128), 0u, RLX_AGENT);
    }
    const unsigned f0 = echo_of(yo), old = chal_of(yi);
    const unsigned chal = (f0 + (((old - f0 - 1u) & 0xffffffu) < 2u ? 3u : 1u)) & 0xffffffu;
    auto tile = [&](f32x4& r0, f32x4& r1) {
      const int q = n >> 2, p = n & 3;
      LAS char* wp = lds + L_W + (8 * c + q) * 128 + (gu ? 16 * p : 8 * p);
      const int o1 = gu ? 8 : 32, o2 = 64;
      const LAS char* ap = lds + L_ACT + (n & 7) * ACT_LD + 16 * c;
      f32x4 d[4];
      for (int T = 0; T < 4; ++T) d[T] = f32x4{0.f, 0.f, 0.f, 0.f};
#pragma unroll 2
      for (int s = 0; s < 18; ++s) {
        const u32x4 av = *(const LAS u32x4*)(ap + 64 * s);
        for (int T = 0; T < 4; ++T) {
          LAS char* a = wp + s * 4096 + (T & 1) * o1 + (T >> 1) * o2;
          const u32x2 lo = trread(a), hi2 = trread(a + 512);
          d[T] = mfma16(u32x4{lo[0], lo[1], hi2[0], hi2[1]}, av, d[T]);
        }
      }
      if (gu) { r0 = n < 8 ? d[0] : d[2]; r1 = n < 8 ? d[1] : d[3]; }
      else { r0 = n < 8 ? d[0] : d[1]; r1 = n < 8 ? d[2] : d[3]; }
    };
    u32x4 xd[9];
    if (gu) for (int t = 0; t < 9; ++t) xd[t] = *(const u32x4*)(x + (lane >> 3) * DM + 8 * ((lane & 7) + 8 * t));
    vmwait<0>();
    if (blockIdx.x != LEADER && lane == 0) __hip_atomic_store(hi + blockIdx.x, word(chal_of(yo), chal, tent), RLX_AGENT);
    if (gu) {
      float sm = 0.f, sq = 0.f;
      for (int t = 0; t < 9; ++t) for (int e = 0; e < 4; ++e) sm += bflo(xd[t][e]) + bfhi(xd[t][e]);
      sm += dppf<XOR1>(sm); sm += dppf<XOR2>(sm); sm += dppf<XOR7>(sm);
      const float mean = sm * (1.0f / DM);
      for (int t = 0; t < 9; ++t) for (int e = 0; e < 4; ++e) { const float a = bflo(xd[t][e]) - mean, d = bfhi(xd[t][e]) - mean; sq += a * a + d * d; }
      sq += dppf<XOR1>(sq); sq += dppf<XOR2>(sq); sq += dppf<XOR7>(sq);
      const LnStat st = ln_finish(mean, sq * (1.0f / DM));
      for (int t = 0; t < 9; ++t) {
        u32x4 v;
        for (int e = 0; e < 4; ++e) v[e] = pack2(ln_apply(bflo(xd[t][e]), st), ln_apply(bfhi(xd[t][e]), st));
        *(LAS u32x4*)(lds + L_ACT + (lane >> 3) * ACT_LD + 16 * ((lane & 7) + 8 * t)) = v;
      }
      f32x4 r0, r1;
      tile(r0, r1);
      const u32x4 o = {pack2(r0[0], r0[1]), pack2(r0[2], r0[3]), pack2(r1[0], r1[1]), pack2(r1[2], r1[3])};
      __builtin_amdgcn_raw_buffer_store_b128(o, rw, WS_GU + ((mat * NB + (n & 7)) * FF + 64 * u + 32 * (n >> 3) + 8 * c) * 2, 0, SC1);
      vmwait<0>();
      if (lane == 0) __hip_atomic_store(flag, 1u, RLX_AGENT);
    }
    if (blockIdx.x == LEADER) {
      unsigned long long y[4]; unsigned cl[4], sent[4];
      gate(G_L1);
      for (int q = 0; q < 4; ++q) { y[q] = __hip_atomic_load(hi + q * 64 + lane, RLX_AGENT); cl[q] = (echo_of(y[q]) + 1u) & 0xffffffu; sent[q] = ~0u; }
      SPIN(SPIN_CAP) {
        bool ok = true;
        for (int q = 0; q < 4; ++q) ok = ok && (echo_of(y[q]) == cl[q] || q * 64 + lane == LEADER);
        if (__all(ok)) break;
        for (int q = 0; q < 4; ++q)
          if (sent[q] != chal_of(y[q])) { sent[q] = chal_of(y[q]); __hip_atomic_store(ho + q * 64 + lane, word(sent[q], cl[q], 0u), RLX_AGENT); }
        gate(spins == 0 ? G_L1 + 1 : G_L2);
        __builtin_amdgcn_s_sleep(32);
        for (int q = 0; q < 4; ++q) y[q] = __hip_atomic_load(hi + q * 64 + lane, RLX_AGENT);
      }
      float ls = 0.f;
      for (int q = 0; q < 4; ++q) ls += q * 64 + lane == LEADER ? 0.f : (float)(short)((unsigned)(y[q] >> 48) - tent);
      const int mean = (int)(wave_sum(ls) * (1.0f / 255.f));
      for (int q = 0; q < 4; ++q) __hip_atomic_store(ho + q * 64 + lane, word(chal_of(y[q]) + 1u, cl[q], mean + tent), RLX_AGENT);
      ctl[C_SHIFT] = mean;
    } else {
      gate(G_HS);
      SPIN(SPIN_CAP) {
        const unsigned long long y = __hip_atomic_load(ho + blockIdx.x, RLX_AGENT);
        if (((__builtin_amdgcn_readfirstlane(echo_of(y)) - chal) & 0xffffffu) < 2u) {
          if (lane == 0) __hip_atomic_store(hi + blockIdx.x, word(chal_of(y), chal, ctl[C_LAG] + tent), RLX_AGENT);
          break;
        }
        __builtin_amdgcn_s_sleep(64);
      }
      gate(G_GO);
      SPIN(SPIN_CAP) {
        const unsigned long long y = __hip_atomic_load(ho + blockIdx.x, RLX_AGENT);
        if (__builtin_amdgcn_readfirstlane(echo_of(y)) == ((chal + 1u) & 0xffffffu)) { ctl[C_SHIFT] = (int)(short)((unsigned)(y >> 48) - tent); break; }
        __builtin_amdgcn_s_sleep(64);
      }
    }
    asm volatile("" ::: "memory");
    if (dn) {
      gate(G_D);
      gu32* fg = (gu32*)(ws + WS_FG) + 18 * xg + (lane < 18 ? lane : 0);
      SPIN(SPIN_CAP) { if (__all(__hip_atomic_load(fg, RLX_AGENT) == 1u)) break; __builtin_amdgcn_s_sleep(64); }
      asm volatile("" ::: "memory");
#pragma unroll 1
      for (int t3 = 0; t3 < 3; ++t3) {
        u32x4 gv[3], uv[3]; int lo[3];
        for (int t = 0; t < 3; ++t) {
          const int q = lane + 64 * (3 * t3 + t), bb = q / 72, ch = q - 72 * bb, go = WS_GU + (bb * FF + 576 * xg + 8 * ch) * 2;
          gv[t] = __builtin_amdgcn_raw_buffer_load_b128(rw, go, 0, SC1); uv[t] = __builtin_amdgcn_raw_buffer_load_b128(rw, go + NB * FF * 2, 0, SC1);
          lo[t] = L_ACT + bb * ACT_LD + 16 * ch;
        }
        for (int t = 0; t < 3; ++t) {
          u32x4 hv;
          for (int e = 0; e < 4; ++e) hv[e] = pack2(rbf(silu(bflo(gv[t][e]))) * bflo(uv[t][e]), rbf(silu(bfhi(gv[t][e]))) * bfhi(uv[t][e]));
          *(LAS u32x4*)(lds + lo[t]) = hv;
        }
      }
      f32x4 r0, r1;
      tile(r0, r1);
      const int fo = WS_PF + ((xg * NB + (n & 7)) * DM + 64 * hb + 16 * (n >> 3) + 4 * c) * 4;
      __builtin_amdgcn_raw_buffer_store_b128(__builtin_bit_cast(u32x4, r0), rw, fo, 0, SC1);
      __builtin_amdgcn_raw_buffer_store_b128(__builtin_bit_cast(u32x4, r1), rw, fo + 128, 0, SC1);
      vmwait<0>();
      if (lane == 0) __hip_atomic_store(flag, 1u, RLX_AGENT);
    }
    const int cons = (h & 1) == 0 ? (h >> 1) : (h < 4 ? 16 + (h >> 1) : -1);
    const int pl = lane >> 3, col = 32 * cons + 4 * (lane & 7);
    const int ocol = b * DM + col + 2 * (c & 1) + (n >> 3);
    float xx = 0.f, gg = 0.f;
#pragma unroll 1
    for (int r = 0; r < NRND; ++r) {
      const unsigned blk = RND0 + RNDS * r;
      gate(blk);
      if (ri >= 0 && lane == 0) __hip_atomic_store((gu32*)(ws + WS_RP + r * 128 + (xg * 4 + ri) * 4), ((ctl[C_LAG] + tent) & 0xffffu) | 0x10000u, RLX_AGENT);
      if (blockIdx.x == LEADER) {
        gate(blk + 1);
        const unsigned v = __hip_atomic_load((gu32*)(ws + WS_RP + r * 128 + (lane & 31) * 4), RLX_AGENT);
        const float cnt = wave_sum(v != 0u ? 1.f : 0.f), sum = wave_sum(v != 0u ? (float)(short)(v - tent) : 0.f);
        if (cnt >= 32.f && lane < 8) __hip_atomic_store((gu32*)(ws + WS_BC + (r * 8 + lane) * 128), (((int)(sum / cnt) + tent) & 0xffffu) | 0x10000u, RLX_AGENT);
      }
      if (r == RND_F && cons >= 0) {
        xx = bflo(x[ocol]);
        gu32* fp = (gu32*)(ws + WS_FP) + 9 * pl + (cons >> 1);
        SPIN(SPIN_CAP) { if (__all(__hip_atomic_load(fp, RLX_AGENT) == 1u)) break; __builtin_amdgcn_s_sleep(64); }
        asm volatile("" ::: "memory");
        gg = rbf(silu(rbf(reduce8(__builtin_bit_cast(f32x4, __builtin_amdgcn_raw_buffer_load_b128(rw, WS_PF + ((pl * NB + b) * DM + col) * 4, 0, SC1)), n & 8))));
      }
      gate(blk + 2);
      const unsigned m = __hip_atomic_load((gu32*)(ws + WS_BC + (r * 8 + xg) * 128), RLX_AGENT);
      if (m != 0u) ctl[C_SHIFT] = (int)(short)(m - tent);
    }
    if (cons < 0) { __syncthreads(); return; }
    __syncthreads();
    __builtin_amdgcn_s_sleep(POLL_DELAY);
    u32x4 pv[4];
    int po[4];
    for (int t = 0; t < 4; ++t) { pv[t] = u32x4{SENT, SENT, SENT, SENT}; po[t] = WS_P + ((b * NH + 8 * t + pl) * DM + col) * 4; }
    SPIN(16 * SPIN_CAP) {
      asm volatile("" ::: "memory");
      u32x4 v[4];
      for (int t = 0; t < 4; ++t) v[t] = __builtin_amdgcn_raw_buffer_load_b128(rw, po[t], 0, SC1);
      bool ok = true;
      for (int t = 0; t < 4; ++t) {
        const bool had = po[t] == WS_END, got = v[t][0] != SENT && v[t][1] != SENT && v[t][2] != SENT && v[t][3] != SENT;
        for (int r = 0; r < 4; ++r) pv[t][r] = had ? pv[t][r] : v[t][r];
        po[t] = got ? WS_END : po[t];
        ok = ok && got;
      }
      if (__all(ok)) break;
    }
    f32x4 s4;
    for (int r = 0; r < 4; ++r) s4[r] = (__uint_as_float(pv[0][r]) + __uint_as_float(pv[1][r])) + (__uint_as_float(pv[2][r]) + __uint_as_float(pv[3][r]));
    const float o = xx + rbf(rbf(reduce8(s4, n & 8)) * gg);
    if (lane < 32) out[ocol] = (u16)pack2(o, 0.f);
  }
}

extern "C" void kernel_launch(void* const* d_in, const int* in_sizes, int n_in, void* d_out, int out_size, void* d_ws, size_t ws_size, hipStream_t stream) {
  k_main<<<256, 512, 0, stream>>>((const u16*)d_in[0], (const u16*)d_in[1], (const u16*)d_in[2], (const u16*)d_in[3], (const u16*)d_in[4], (const u16*)d_in[6],
                                  (const u16*)d_in[7], (const u16*)d_in[8], (char*)d_ws, (u16*)d_out);
}
```

```cpp
#define PROBE 0
#define PWG 100
#define PMUL 10
#include <hip/hip_runtime.h>
#include <stdint.h>

typedef unsigned short u16;
typedef unsigned u32x4 __attribute__((ext_vector_type(4)));
typedef unsigned u32x2 __attribute__((ext_vector_type(2)));
typedef float f32x4 __attribute__((ext_vector_type(4)));
typedef short s16x4 __attribute__((ext_vector_type(4)));
typedef __bf16 bf16x8 __attribute__((ext_vector_type(8)));
typedef __bf16 bf16x2 __attribute__((ext_vector_type(2)));
#define LAS __attribute__((address_space(3)))
typedef __attribute__((address_space(1))) unsigned gu32;
typedef __attribute__((address_space(1))) unsigned long long gu64;
#define RLX_AGENT __ATOMIC_RELAXED, __HIP_MEMORY_SCOPE_AGENT

constexpr int NB = 8, DM = 576, NH = 32, SK = 4096, DH = 128, FF = 4608, HD = NH * DH;

constexpr int PK0 = 6, PKW = 4, PQW = 5, FFW = 7;
constexpr int XP0 = 8, XP1 = 2;
constexpr int WO_T0 = 1500, WO_DT = 110, WO_BU = 3;
constexpr int PC_T3 = 1385, PC_RK = 225, PC_RV = 221;
constexpr int PC_L0 = 20, PC_G = 16;
constexpr int G_L1 = 5, G_HS = 7, G_L2 = 8, G_GO = 10, G_D = 11;
constexpr int NRND = 5, RND0 = 11, RNDS = 4, RND_F = 2;
constexpr int POLL_DELAY = 40;
constexpr int WIN = 7;
constexpr int NPRE = 2;
constexpr int XPRE = XP0 + NPRE * XP1;

constexpr int WS_P = 0, WS_PF = WS_P + NB * NH * DM * 4, WS_GU = WS_PF + 8 * NB * DM * 4, WS_FG = WS_GU + 2 * NB * FF * 2, WS_FP = WS_FG + 1024,
              WS_HI = WS_FP + 1024, WS_HO = WS_HI + 256 * 8, WS_RP = WS_HO + 256 * 8, WS_BC = WS_RP + NRND * 128,
              WS_END = WS_BC + NRND * 8 * 128;
constexpr int LEADER = 255;
constexpr unsigned SENT = 0xffffffffu;
constexpr unsigned SPIN_CAP = 1u << 18, LDS_CAP = 1u << 22;
constexpr int ACT_LD = DM * 2 + 16;
constexpr int L_W = 0;
constexpr int L_ACT = L_W + DM * 128;
constexpr int L_SC = L_ACT + NB * ACT_LD;
constexpr int L_QP = L_SC + 4 * 16 * 64 * 2;
constexpr int L_MRG = L_QP + 3 * 128 * 4;
constexpr int L_CTL = L_MRG + 4 * 132 * 4;
constexpr int L_END = L_CTL + 32;
enum { C_Q = 0, C_BLK = 1, C_ML = 2, C_LAG = 3, C_SHIFT = 4 };

template <int I> struct IC { static constexpr int v = I; };
template <int N, int I = 0, typename Fn> __device__ __forceinline__ void sfor(Fn&& f) {
  if constexpr (I < N) { f(IC<I>{}); sfor<N, I + 1>(f); }
}
#define SCHED() __builtin_amdgcn_sched_barrier(0)
#define SPIN(cap) _Pragma("unroll 1") for (unsigned spins = 0; spins < (cap); ++spins)
template <int N> __device__ __forceinline__ void vmwait() { asm volatile("s_waitcnt vmcnt(%0)" ::"n"(N) : "memory"); }
template <int I> __device__ __forceinline__ void vmwait_l(int lvl) {
  constexpr int br = ((I & 1) << 3) | ((I & 2) << 1) | ((I & 4) >> 1) | ((I & 8) >> 3);
  asm volatile("s_cmp_gt_i32 %0, %1\n\ts_cbranch_scc1 3f\n\ts_cmp_gt_i32 %0, %2\n\ts_cbranch_scc1 2f\n\ts_cmp_gt_i32 %0, %3\n\ts_cbranch_scc1 1f\n\t"
               "s_waitcnt vmcnt(3)\n1:\n\ts_waitcnt vmcnt(4)\n2:\n\ts_waitcnt vmcnt(5)\n3:\n\ts_waitcnt vmcnt(6)"
               ::"s"(lvl), "n"(32 + br), "n"(16 + br), "n"(br) : "memory", "scc");
}
__device__ __forceinline__ unsigned now() { return (unsigned)__builtin_amdgcn_s_memrealtime(); }

__device__ __forceinline__ float bflo(unsigned x) { return __uint_as_float(x << 16); }
__device__ __forceinline__ float bfhi(unsigned x) { return __uint_as_float(x & 0xffff0000u); }
__device__ __forceinline__ unsigned pack2(float lo, float hi) { bf16x2 v = {(__bf16)lo, (__bf16)hi}; return __builtin_bit_cast(unsigned, v); }
__device__ __forceinline__ float rbf(float x) { return bflo((unsigned)__builtin_bit_cast(u16, (__bf16)x)); }
template <int CTRL> __device__ __forceinline__ float dppf(float x) {
  return __builtin_bit_cast(float, __builtin_amdgcn_mov_dpp(__builtin_bit_cast(int, x), CTRL, 0xf, 0xf, true));
}
constexpr int XOR1 = 0xB1, XOR2 = 0x4E, XOR7 = 0x141, XOR8 = 0x128, UP4 = 0x104, DOWN4 = 0x114;
template <int N> __device__ __forceinline__ unsigned bcast16(unsigned x) {
  return (unsigned)__builtin_amdgcn_mov_dpp((int)x, 0x150 + N, 0xf, 0xf, true);
}
__device__ __forceinline__ float x16(float x, bool mx) {
  auto s = __builtin_amdgcn_permlane16_swap(__float_as_uint(x), __float_as_uint(x), false, false);
  x = mx ? fmaxf(__uint_as_float(s[0]), __uint_as_float(s[1])) : __uint_as_float(s[0]) + __uint_as_float(s[1]);
  auto t = __builtin_amdgcn_permlane32_swap(__float_as_uint(x), __float_as_uint(x), false, false);
  return mx ? fmaxf(__uint_as_float(t[0]), __uint_as_float(t[1])) : __uint_as_float(t[0]) + __uint_as_float(t[1]);
}
__device__ __forceinline__ float wave_sum(float v) {
  v += dppf<XOR1>(v); v += dppf<XOR2>(v); v += dppf<XOR7>(v); v += dppf<XOR8>(v); return x16(v, false);
}
__device__ __forceinline__ float wave_max(float v) {
  v = fmaxf(v, dppf<XOR1>(v)); v = fmaxf(v, dppf<XOR2>(v)); v = fmaxf(v, dppf<XOR7>(v)); v = fmaxf(v, dppf<XOR8>(v)); return x16(v, true);
}
__device__ __forceinline__ float hi_half(float x) {
  auto t = __builtin_amdgcn_permlane32_swap(__float_as_uint(x), __float_as_uint(x), false, false);
  return __uint_as_float(t[1]);
}
template <int CH, int CL> __device__ __forceinline__ float tmerge(float lo, float hi, bool b) {
  const float u = hi + dppf<CH>(hi), v = lo + dppf<CL>(lo);
  return b ? u : v;
}
__device__ __forceinline__ float reduce8(const f32x4& v, bool b3) {
  const float u = tmerge<XOR8, XOR8>(v[0], v[1], b3), t = tmerge<XOR8, XOR8>(v[2], v[3], b3);
  auto a = __builtin_amdgcn_permlane16_swap(__float_as_uint(u), __float_as_uint(t), false, false);
  const float z = __uint_as_float(a[0]) + __uint_as_float(a[1]);
  auto e = __builtin_amdgcn_permlane32_swap(__float_as_uint(z), __float_as_uint(z), false, false);
  return __uint_as_float(e[0]) + __uint_as_float(e[1]);
}
__device__ __forceinline__ void pin(float& x) { asm volatile("" : "+v"(x)); }
__device__ __forceinline__ f32x4 mfma16(u32x4 a, u32x4 b, f32x4 c) {
  return __builtin_amdgcn_mfma_f32_16x16x32_bf16(__builtin_bit_cast(bf16x8, a), __builtin_bit_cast(bf16x8, b), c, 0, 0, 0);
}
__device__ __forceinline__ float dot2(unsigned a, unsigned b, float c) {
  return __builtin_amdgcn_fdot2_f32_bf16(__builtin_bit_cast(bf16x2, a), __builtin_bit_cast(bf16x2, b), c, false);
}
__device__ __forceinline__ float silu(float x) { return x / (1.0f + __expf(-x)); }
__device__ __forceinline__ u32x4 diag(unsigned pk, const u32x4& sel) {
  u32x4 a;
  for (int i = 0; i < 4; ++i) a[i] = __builtin_amdgcn_perm(0u, pk, sel[i]);
  return a;
}
__device__ __forceinline__ auto rsrc(const void* p, int bytes) { return __builtin_amdgcn_make_buffer_rsrc((void*)p, 0, bytes, 0x00020000); }
constexpr int NT = 2, SC1 = 16;
__device__ __forceinline__ void glds16(const void* gsrc, unsigned lds_dst) {
  unsigned keep;
  asm volatile("s_mov_b32 %0, m0\n\ts_mov_b32 m0, %2\n\ts_nop 0\n\tglobal_load_lds_dwordx4 %1, off nt\n\ts_mov_b32 m0, %0" : "=&s"(keep) : "v"(gsrc), "s"(lds_dst) : "memory");
}
__device__ __forceinline__ u32x2 trread(LAS char* p) { return __builtin_bit_cast(u32x2, __builtin_amdgcn_ds_read_tr16_b64_v4i16((LAS s16x4*)p)); }

struct LnStat { float m, den, inv; };
__device__ __forceinline__ LnStat ln_finish(float mean, float var) {
  const float den = rbf(sqrtf(rbf(rbf(var) + 1e-5f)));
  return LnStat{rbf(mean), den, 1.0f / den};
}
__device__ __forceinline__ float ln_apply(float x, const LnStat& s) {
  const float a = rbf(x - s.m), q = a * s.inv;
  return fmaf(fmaf(-q, s.den, a), s.inv, q);
}

__global__ void __launch_bounds__(512, 2) k_main(const u16* __restrict__ x, const u16* __restrict__ K, const u16* __restrict__ V, const u16* __restrict__ wq,
                                                 const u16* __restrict__ wo, const u16* __restrict__ w1, const u16* __restrict__ w3,
                                                 const u16* __restrict__ w2, char* ws, u16* __restrict__ out) {
  __shared__ __attribute__((aligned(16))) char lds_[L_END];
  LAS char* lds = (LAS char*)lds_;
  const unsigned tent = now();
  unsigned pm = 0;
#define PRB(k) do { if (PROBE == (k)) pm = (now() - tent) * PMUL; } while (0)
#define PRBWAIT() do { if (PWG < 0 || (int)blockIdx.x == PWG) while (now() - tent < 10000u + pm) __builtin_amdgcn_s_sleep(1); } while (0)
  asm volatile("" ::"s"(x), "s"(K), "s"(V), "s"(wq), "s"(w1), "s"(w3), "s"(w2), "s"(ws));
  SCHED(); PRB(6); SCHED();
  const int tid = threadIdx.x, lane = tid & 63, w = __builtin_amdgcn_readfirstlane(tid >> 6);
  const int xg = blockIdx.x & 7, j0 = blockIdx.x >> 3, b = j0 >> 2;
  const int h = (j0 & 1) | (xg << 1) | ((j0 & 2) << 3), j = (j0 & 1) * 16 + (j0 >> 1);
  const char* Kb = (const char*)(K + (size_t)(b * NH + h) * SK * DH);
  const int voff = lane * 16 + w * 4096;
  u32x4 ring[16];
  auto ld = [&](const char* blk, auto ic) { constexpr int i = ic.v;
    ring[i] = __builtin_amdgcn_raw_buffer_load_b128(rsrc(blk, 65536), voff + (i & 3) * 1024, (i >> 2) * 16384, NT); };
  if (w < 4) sfor<PK0>([&](auto ic) { ld(Kb, ic); });
  SCHED();
  PRB(7);
  const int c = lane >> 4, n = lane & 15;
  volatile LAS unsigned* ctl = (volatile LAS unsigned*)(lds + L_CTL);
  if (tid < 8) ctl[tid] = 0u;
  __syncthreads();
  u32x4 sel;
  {
    const unsigned src = (n < 8) ? 0x0100u : 0x0302u; const int jj = n & 7;
    for (int i = 0; i < 4; ++i) sel[i] = ((jj >> 1) != i) ? 0x0c0c0c0cu : ((jj & 1) ? (0x0c0cu | (src << 16)) : (0x0c0c0000u | src));
  }
  if (w < 4) {
    const char* Vb = (const char*)(V + (size_t)(b * NH + h) * SK * DH);
    u32x4 pre[16 * NPRE];
    vmwait<PK0 - 1>(); PRB(8);
    sfor<16 - PK0>([&](auto ic) { SCHED(); vmwait<PKW - 1>(); SCHED(); ld(Kb, IC<ic.v + PK0>{}); });
    sfor<16 * NPRE>([&](auto ic) { constexpr int i = ic.v; SCHED(); vmwait<PKW - 1>(); SCHED();
      pre[i] = __builtin_amdgcn_raw_buffer_load_b128(rsrc(Kb + 65536, NPRE * 65536), voff + (i & 3) * 1024, (i >> 2) * 16384, NT); });
    SCHED();
    PRB(9);
    __builtin_amdgcn_s_setprio(3);
    SPIN(LDS_CAP) { if (ctl[C_Q] >= 3u) break; __builtin_amdgcn_s_sleep(1); }
    asm volatile("" ::: "memory");
    unsigned qd[4];
    {
      f32x4 a = {0.f, 0.f, 0.f, 0.f}, e = a;
      for (int i = 0; i < 3; ++i) { a += *(LAS f32x4*)(lds + L_QP + i * 512 + 32 * n); e += *(LAS f32x4*)(lds + L_QP + i * 512 + 32 * n + 16); }
      qd[0] = pack2(a[0], a[1]); qd[1] = pack2(a[2], a[3]); qd[2] = pack2(e[0], e[1]); qd[3] = pack2(e[2], e[3]);
    }
    LAS u16* sc = (LAS u16*)(lds + L_SC) + w * 1024 + lane;
    float mx = -INFINITY;
    int lvl = PC_L0;
    unsigned due = tent + (PC_T3 - PC_RK);
    auto pace = [&](int ticks) {
      due += ticks;
      const int shift = __builtin_amdgcn_readfirstlane(ctl[C_SHIFT]);
      const int lag = min(max((int)(now() - due), -4096), 4096);
      if (w == 0) ctl[C_LAG] = lag;
      lvl = min(max(PC_L0 + ((lag - shift) * PC_G >> 6), 0), 48);
    };
    const bool b0 = n & 1, b1 = n & 2, b2 = n & 4, b3 = n & 8;
    auto kblock = [&](const char* cur, const char* nxt, int blk, auto mode) {
      float t0, t1, t2, t3;
      sfor<16>([&](auto ic) { constexpr int i = ic.v;
        const u32x4 kv = mode.v >= 2 ? pre[(mode.v >= 2 ? mode.v - 2 : 0) * 16 + i] : ring[i];
        if constexpr (mode.v == 0 || (mode.v == -1 && i + WIN >= XPRE)) {
          SCHED(); vmwait_l<i>(lvl); SCHED(); if constexpr (i + WIN >= 16) ld(nxt, IC<i + WIN - 16>{}); else ld(cur, IC<i + WIN>{}); SCHED(); }
        float d = dot2(kv[0], qd[0], 0.f); d = dot2(kv[1], qd[1], d); d = dot2(kv[2], qd[2], d); d = dot2(kv[3], qd[3], d);
        pin(d);
        SCHED();
        if constexpr (mode.v == 1 && i < XP0) { ld(nxt, ic); SCHED(); }
        if constexpr (mode.v >= 2 && i % (16 / XP1) == 0) { ld(nxt, IC<XP0 + (mode.v - 2) * XP1 + i / (16 / XP1)>{}); SCHED(); }
        if constexpr (!(i & 1)) t0 = d;
        else {
          d = tmerge<XOR1, XOR1>(t0, d, b0);
          if constexpr (!(i & 2)) t1 = d;
          else {
            d = tmerge<XOR2, XOR2>(t1, d, b1);
            if constexpr (!(i & 4)) t2 = d;
            else {
              d = tmerge<DOWN4, UP4>(t2, d, b2);
              if constexpr (!(i & 8)) t3 = d;
              else {
                d = tmerge<XOR8, XOR8>(t3, d, b3);
                d = rbf(rbf(d) * 0.08838834764831845f);
                mx = fmaxf(mx, d);
                sc[blk * 64] = (u16)(__float_as_uint(d) >> 16);
                if (w == 0) ctl[C_BLK] = blk + 1;
              }
            }
          }
        }
      });
    };
    constexpr int B0 = NPRE + 1;
    kblock(Kb, Kb + B0 * 65536, 0, IC<1>{});
    sfor<NPRE>([&](auto pc) { kblock(Kb, Kb + B0 * 65536, 1 + pc.v, IC<2 + pc.v>{}); });
    kblock(Kb + B0 * 65536, Kb + (B0 + 1) * 65536, B0, IC<-1>{});
    pace(PC_RK);
#pragma unroll 1
    for (int blk = B0 + 1; blk < 16; ++blk) { kblock(Kb + blk * 65536, blk < 15 ? Kb + (blk + 1) * 65536 : Vb, blk, IC<0>{}); pace(PC_RK); }
    mx = wave_max(mx);
    float l = 0.f;
    f32x4 acc = {0.f, 0.f, 0.f, 0.f};
    float s = bflo(sc[0]);
    LAS float* mr = (LAS float*)(lds + L_MRG) + w * 132;
    auto vblock = [&](const char* cur, const char* nxt, int blk, auto last) {
      if (w == 0) ctl[C_BLK] = 16 + blk;
      const float p = __expf(s - mx);
      l += p;
      const float ph = rbf(p);
      const unsigned pk = pack2(ph, p - ph);
      if constexpr (!last.v) s = bflo(sc[(blk + 1) * 64]);
      else {
        l = wave_sum(l);
        if (lane == 0) { mr[0] = mx; mr[1] = l; }
        asm volatile("s_waitcnt lgkmcnt(0)" ::: "memory");
        if (lane == 0) __hip_atomic_fetch_add((LAS unsigned*)(lds + L_CTL) + C_ML, 1u, __ATOMIC_RELAXED, __HIP_MEMORY_SCOPE_WORKGROUP);
      }
      sfor<16>([&](auto ic) { constexpr int i = ic.v;
        const u32x4 a = diag(bcast16<i>(pk), sel), vv = ring[i];
        SCHED();
        if constexpr (!last.v || i + WIN < 16) vmwait_l<i>(lvl);
        SCHED();
        if constexpr (i + WIN < 16) ld(cur, IC<i + WIN>{}); else if constexpr (!last.v) ld(nxt, IC<i + WIN - 16>{});
        SCHED();
        acc = mfma16(a, vv, acc);
        SCHED();
      });
    };
#pragma unroll 1
    for (int blk = 0; blk < 15; ++blk) { vblock(Vb + blk * 65536, Vb + (blk + 1) * 65536, blk, IC<0>{}); pace(PC_RV); }
    vblock(Vb + 15 * 65536, Vb, 15, IC<1>{});
    for (int r = 0; r < 2; ++r) {
      auto t = __builtin_amdgcn_permlane32_swap(__float_as_uint(acc[r]), __float_as_uint(acc[r + 2]), false, false);
      mr[4 + (4 * (c & 1) + 2 * (c >> 1) + r) * 16 + n] = __uint_as_float(t[0]) + __uint_as_float(t[1]);
    }
    __syncthreads();
    if (w == 0) PRBWAIT();
  } else if (w < 7) {
    const int i4 = w - 4;
    {
      const unsigned* xr = (const unsigned*)(x + b * DM);
      unsigned d[5];
      for (int t = 0; t < 4; ++t) d[t] = xr[lane + 64 * t];
      d[4] = xr[256 + (lane & 31)];
      const u16* xe = x + b * DM + 192 * i4 + c;
      const unsigned e0 = xe[8 * n], e1 = xe[8 * n + 4], e2 = xe[128 + 8 * (n & 7)], e3 = xe[128 + 8 * (n & 7) + 4];
      SCHED();
      u32x4 wr[48];
      const auto rq = rsrc(wq, DM * HD * 2);
      const int vo = (4 * (n >> 3) + c) * (HD * 2) + (n & 7) * 16;
      const int so = 192 * i4 * (HD * 2) + h * 256;
      auto ldq = [&](auto ic) { constexpr int i = ic.v;
        if constexpr (i < 48) wr[i] = __builtin_amdgcn_raw_buffer_load_b128(rq, vo + (i & 1) * 128, so + (i >> 1) * 8 * (HD * 2), NT); };
      sfor<PQW>(ldq);
      SCHED();
      float sm = 0.f, sq = 0.f;
      for (int t = 0; t < 5; ++t) sm += (t < 4 || lane < 32) ? bflo(d[t]) + bfhi(d[t]) : 0.f;
      const float mean = wave_sum(sm) * (1.0f / DM);
      for (int t = 0; t < 5; ++t) { const float a = bflo(d[t]) - mean, e = bfhi(d[t]) - mean; sq += (t < 4 || lane < 32) ? a * a + e * e : 0.f; }
      const LnStat st = ln_finish(mean, wave_sum(sq) * (1.0f / DM));
      const unsigned pk0 = pack2(ln_apply(bflo(e0), st), ln_apply(bflo(e1), st)), pk1 = pack2(ln_apply(bflo(e2), st), ln_apply(bflo(e3), st));
      f32x4 a0 = {0.f, 0.f, 0.f, 0.f}, a1 = a0;
      sfor<24>([&](auto sc_) { constexpr int s = sc_.v;
        const u32x4 a = diag(bcast16<s & 15>(s < 16 ? pk0 : pk1), sel);
        a0 = mfma16(a, wr[2 * s], a0); SCHED(); ldq(IC<2 * s + PQW>{}); SCHED();
        a1 = mfma16(a, wr[2 * s + 1], a1); SCHED(); ldq(IC<2 * s + 1 + PQW>{}); SCHED(); });
      f32x4 o0, o1;
      for (int r = 0; r < 4; ++r) { o0[r] = a0[r] + hi_half(dppf<XOR8>(a0[r])); o1[r] = a1[r] + hi_half(dppf<XOR8>(a1[r])); }
      if (c < 2 && n < 8) { *(LAS f32x4*)(lds + L_QP + i4 * 512 + 32 * n + 16 * c) = o0; *(LAS f32x4*)(lds + L_QP + i4 * 512 + 256 + 32 * n + 16 * c) = o1; }
      asm volatile("s_waitcnt lgkmcnt(0)" ::: "memory");
      if (lane == 0) __hip_atomic_fetch_add((LAS unsigned*)(lds + L_CTL) + C_Q, 1u, __ATOMIC_RELAXED, __HIP_MEMORY_SCOPE_WORKGROUP);
    }
    u32x4 wr[48];
    const auto ro = rsrc(wo, HD * DM * 2);
    const int vo = (4 * (n >> 3) + c) * (DM * 2) + (n & 7) * 16;
    const int so = 128 * h * (DM * 2) + 384 * i4;
    sfor<48>([&](auto ic) { constexpr int i = ic.v;
      if constexpr (i % WO_BU == 0) SPIN(LDS_CAP) { if (now() - tent >= (unsigned)(WO_T0 + i * WO_DT)) break; __builtin_amdgcn_s_sleep(2); }
      wr[i] = __builtin_amdgcn_raw_buffer_load_b128(ro, vo + (i % 3) * 128, so + (i / 3) * 8 * (DM * 2), NT);
      SCHED(); if constexpr (i % WO_BU == WO_BU - 1) vmwait<WO_BU>(); SCHED(); });
    const LAS float* mr = (const LAS float*)(lds + L_MRG);
    SPIN(LDS_CAP) { if (ctl[C_ML] >= 4u) break; __builtin_amdgcn_s_sleep(4); }
    asm volatile("" ::: "memory");
    float f4[4], M = -INFINITY, L = 0.f;
    for (int i = 0; i < 4; ++i) { f4[i] = mr[i * 132]; M = fmaxf(M, f4[i]); }
    for (int i = 0; i < 4; ++i) { f4[i] = __expf(f4[i] - M); L += f4[i] * mr[i * 132 + 1]; }
    float linv = 1.0f / L;
    for (int i = 0; i < 4; ++i) pin(f4[i]);
    pin(linv);
    __syncthreads();
    float oa = 0.f, ob = 0.f;
    for (int i = 0; i < 4; ++i) { oa += f4[i] * mr[i * 132 + 4 + c * 16 + n]; ob += f4[i] * mr[i * 132 + 4 + (4 + c) * 16 + n]; }
    const unsigned pk = pack2(oa * linv, ob * linv);
    f32x4 a3[3] = {{0.f, 0.f, 0.f, 0.f}, {0.f, 0.f, 0.f, 0.f}, {0.f, 0.f, 0.f, 0.f}};
    sfor<16>([&](auto sc_) { constexpr int s = sc_.v;
      const u32x4 a = diag(bcast16<s>(pk), sel);
      for (int t = 0; t < 3; ++t) a3[t] = mfma16(a, wr[3 * s + t], a3[t]); });
    const int po = WS_P + ((b * NH + h) * DM + 192 * i4 + 8 * n + 4 * (c & 1)) * 4;
    u32x4 o01, o2;
    for (int r = 0; r < 4; ++r) {
      auto t = __builtin_amdgcn_permlane32_swap(__float_as_uint(a3[0][r]), __float_as_uint(a3[1][r]), false, false);
      const unsigned v = __float_as_uint(__uint_as_float(t[0]) + dppf<XOR8>(__uint_as_float(t[1])));
      o01[r] = v == SENT ? SENT - 1u : v;
    }
    if (n < 8) __builtin_amdgcn_raw_buffer_store_b128(o01, rsrc(ws, WS_END), po + 256 * (c >> 1), 0, SC1);
    for (int r = 0; r < 4; ++r) {
      const unsigned v = __float_as_uint(a3[2][r] + hi_half(dppf<XOR8>(a3[2][r])));
      o2[r] = v == SENT ? SENT - 1u : v;
    }
    if (n < 8 && c < 2) __builtin_amdgcn_raw_buffer_store_b128(o2, rsrc(ws, WS_END), po + 512, 0, SC1);
  } else {
    const auto rw = rsrc(ws, WS_END);
    auto gate = [&](unsigned blocks) { SPIN(LDS_CAP) { if (ctl[C_BLK] >= blocks) break; __builtin_amdgcn_s_sleep(8); } };
    const bool gu = j < 18, dn = j >= 18 && j < 27;
    const int i0 = (14 - xg) & 7, iu = j >= 16 ? i0 : (j >> 1) + ((j >> 1) >= i0);
    const int mat = j & 1, u = 9 * xg + iu, hb = j - 18;
    gu32* flag = (gu32*)(ws + (gu ? WS_FG + (2 * u + mat) * 4 : WS_FP + (xg * 9 + hb) * 4));
    if (j < 27) {
      const int ldw = gu ? FF * 2 : DM * 2;
      const char* src = (gu ? (const char*)((mat ? w3 : w1) + 64 * u) : (const char*)(w2 + (size_t)576 * xg * DM + 64 * hb)) + (lane >> 3) * ldw + (lane & 7) * 16;
#pragma unroll 1
      for (int i = 0; i < 72; ++i) { glds16(src, (unsigned)(size_t)(lds + L_W) + i * 1024); src += 8 * ldw; vmwait<FFW - 1>(); }
    }
    auto echo_of = [](unsigned long long y) { return (unsigned)y & 0xffffffu; };
    auto chal_of = [](unsigned long long y) { return (unsigned)(y >> 24) & 0xffffffu; };
    auto word = [](unsigned echo, unsigned ch, unsigned hint) {
      return (unsigned long long)(echo & 0xffffffu) | ((unsigned long long)ch << 24) | ((unsigned long long)(hint & 0xffffu) << 48); };
    const int ri = j0 == 2 ? 0 : j0 == 12 ? 1 : j0 == 23 ? 2 : j0 == 29 ? 3 : -1;
    gu64* hi = (gu64*)(ws + WS_HI); gu64* ho = (gu64*)(ws + WS_HO);
    const unsigned long long yo = __hip_atomic_load(ho + blockIdx.x, RLX_AGENT), yi = __hip_atomic_load(hi + blockIdx.x, RLX_AGENT);
    {
      const u32x4 e = {SENT, SENT, SENT, SENT};
      for (int t = 0; t < 3; ++t)
        if (t < 2 || lane < 16) __builtin_amdgcn_raw_buffer_store_b128(e, rw, WS_P + (b * NH + h) * DM * 4 + (t * 64 + lane) * 16, 0, SC1);
      if (lane == 0 && j < 27) __hip_atomic_store(flag, 0u, RLX_AGENT);
      if (ri >= 0 && lane < NRND) __hip_atomic_store((gu32*)(ws + WS_RP + lane * 128 + (xg * 4 + ri) * 4), 0u, RLX_AGENT);
      if (blockIdx.x == LEADER && lane < NRND * 8) __hip_atomic_store((gu32*)(ws + WS_BC + lane * 128), 0u, RLX_AGENT);
    }
    const unsigned f0 = echo_of(yo), old = chal_of(yi);
    const unsigned chal = (f0 + (((old - f0 - 1u) & 0xffffffu) < 2u ? 3u : 1u)) & 0xffffffu;
    auto tile = [&](f32x4& r0, f32x4& r1) {
      const int q = n >> 2, p = n & 3;
      LAS char* wp = lds + L_W + (8 * c + q) * 128 + (gu ? 16 * p : 8 * p);
      const int o1 = gu ? 8 : 32, o2 = 64;
      const LAS char* ap = lds + L_ACT + (n & 7) * ACT_LD + 16 * c;
      f32x4 d[4];
      for (int T = 0; T < 4; ++T) d[T] = f32x4{0.f, 0.f, 0.f, 0.f};
#pragma unroll 2
      for (int s = 0; s < 18; ++s) {
        const u32x4 av = *(const LAS u32x4*)(ap + 64 * s);
        for (int T = 0; T < 4; ++T) {
          LAS char* a = wp + s * 4096 + (T & 1) * o1 + (T >> 1) * o2;
          const u32x2 lo = trread(a), hi2 = trread(a + 512);
          d[T] = mfma16(u32x4{lo[0], lo[1], hi2[0], hi2[1]}, av, d[T]);
        }
      }
      if (gu) { r0 = n < 8 ? d[0] : d[2]; r1 = n < 8 ? d[1] : d[3]; }
      else { r0 = n < 8 ? d[0] : d[1]; r1 = n < 8 ? d[2] : d[3]; }
    };
    u32x4 xd[9];
    if (gu) for (int t = 0; t < 9; ++t) xd[t] = *(const u32x4*)(x + (lane >> 3) * DM + 8 * ((lane & 7) + 8 * t));
    vmwait<0>();
    if (blockIdx.x != LEADER && lane == 0) __hip_atomic_store(hi + blockIdx.x, word(chal_of(yo), chal, tent), RLX_AGENT);
    if (gu) {
      float sm = 0.f, sq = 0.f;
      for (int t = 0; t < 9; ++t) for (int e = 0; e < 4; ++e) sm += bflo(xd[t][e]) + bfhi(xd[t][e]);
      sm += dppf<XOR1>(sm); sm += dppf<XOR2>(sm); sm += dppf<XOR7>(sm);
      const float mean = sm * (1.0f / DM);
      for (int t = 0; t < 9; ++t) for (int e = 0; e < 4; ++e) { const float a = bflo(xd[t][e]) - mean, d = bfhi(xd[t][e]) - mean; sq += a * a + d * d; }
      sq += dppf<XOR1>(sq); sq += dppf<XOR2>(sq); sq += dppf<XOR7>(sq);
      const LnStat st = ln_finish(mean, sq * (1.0f / DM));
      for (int t = 0; t < 9; ++t) {
        u32x4 v;
        for (int e = 0; e < 4; ++e) v[e] = pack2(ln_apply(bflo(xd[t][e]), st), ln_apply(bfhi(xd[t][e]), st));
        *(LAS u32x4*)(lds + L_ACT + (lane >> 3) * ACT_LD + 16 * ((lane & 7) + 8 * t)) = v;
      }
      f32x4 r0, r1;
      tile(r0, r1);
      const u32x4 o = {pack2(r0[0], r0[1]), pack2(r0[2], r0[3]), pack2(r1[0], r1[1]), pack2(r1[2], r1[3])};
      __builtin_amdgcn_raw_buffer_store_b128(o, rw, WS_GU + ((mat * NB + (n & 7)) * FF + 64 * u + 32 * (n >> 3) + 8 * c) * 2, 0, SC1);
      vmwait<0>();
      if (lane == 0) __hip_atomic_store(flag, 1u, RLX_AGENT);
    }
    if (blockIdx.x == LEADER) {
      unsigned long long y[4]; unsigned cl[4], sent[4];
      gate(G_L1);
      for (int q = 0; q < 4; ++q) { y[q] = __hip_atomic_load(hi + q * 64 + lane, RLX_AGENT); cl[q] = (echo_of(y[q]) + 1u) & 0xffffffu; sent[q] = ~0u; }
      SPIN(SPIN_CAP) {
        bool ok = true;
        for (int q = 0; q < 4; ++q) ok = ok && (echo_of(y[q]) == cl[q] || q * 64 + lane == LEADER);
        if (__all(ok)) break;
        for (int q = 0; q < 4; ++q)
          if (sent[q] != chal_of(y[q])) { sent[q] = chal_of(y[q]); __hip_atomic_store(ho + q * 64 + lane, word(sent[q], cl[q], 0u), RLX_AGENT); }
        gate(spins == 0 ? G_L1 + 1 : G_L2);
        __builtin_amdgcn_s_sleep(32);
        for (int q = 0; q < 4; ++q) y[q] = __hip_atomic_load(hi + q * 64 + lane, RLX_AGENT);
      }
      float ls = 0.f;
      for (int q = 0; q < 4; ++q) ls += q * 64 + lane == LEADER ? 0.f : (float)(short)((unsigned)(y[q] >> 48) - tent);
      const int mean = (int)(wave_sum(ls) * (1.0f / 255.f));
      for (int q = 0; q < 4; ++q) __hip_atomic_store(ho + q * 64 + lane, word(chal_of(y[q]) + 1u, cl[q], mean + tent), RLX_AGENT);
      ctl[C_SHIFT] = mean;
    } else {
      gate(G_HS);
      SPIN(SPIN_CAP) {
        const unsigned long long y = __hip_atomic_load(ho + blockIdx.x, RLX_AGENT);
        if (((__builtin_amdgcn_readfirstlane(echo_of(y)) - chal) & 0xffffffu) < 2u) {
          if (lane == 0) __hip_atomic_store(hi + blockIdx.x, word(chal_of(y), chal, ctl[C_LAG] + tent), RLX_AGENT);
          break;
        }
        __builtin_amdgcn_s_sleep(64);
      }
      gate(G_GO);
      SPIN(SPIN_CAP) {
        const unsigned long long y = __hip_atomic_load(ho + blockIdx.x, RLX_AGENT);
        if (__builtin_amdgcn_readfirstlane(echo_of(y)) == ((chal + 1u) & 0xffffffu)) { ctl[C_SHIFT] = (int)(short)((unsigned)(y >> 48) - tent); break; }
        __builtin_amdgcn_s_sleep(64);
      }
    }
    asm volatile("" ::: "memory");
    if (dn) {
      gate(G_D);
      gu32* fg = (gu32*)(ws + WS_FG) + 18 * xg + (lane < 18 ? lane : 0);
      SPIN(SPIN_CAP) { if (__all(__hip_atomic_load(fg, RLX_AGENT) == 1u)) break; __builtin_amdgcn_s_sleep(64); }
      asm volatile("" ::: "memory");
#pragma unroll 1
      for (int t3 = 0; t3 < 3; ++t3) {
        u32x4 gv[3], uv[3]; int lo[3];
        for (int t = 0; t < 3; ++t) {
          const int q = lane + 64 * (3 * t3 + t), bb = q / 72, ch = q - 72 * bb, go = WS_GU + (bb * FF + 576 * xg + 8 * ch) * 2;
          gv[t] = __builtin_amdgcn_raw_buffer_load_b128(rw, go, 0, SC1); uv[t] = __builtin_amdgcn_raw_buffer_load_b128(rw, go + NB * FF * 2, 0, SC1);
          lo[t] = L_ACT + bb * ACT_LD + 16 * ch;
        }
        for (int t = 0; t < 3; ++t) {
          u32x4 hv;
          for (int e = 0; e < 4; ++e) hv[e] = pack2(rbf(silu(bflo(gv[t][e]))) * bflo(uv[t][e]), rbf(silu(bfhi(gv[t][e]))) * bfhi(uv[t][e]));
          *(LAS u32x4*)(lds + lo[t]) = hv;
        }
      }
      f32x4 r0, r1;
      tile(r0, r1);
      const int fo = WS_PF + ((xg * NB + (n & 7)) * DM + 64 * hb + 16 * (n >> 3) + 4 * c) * 4;
      __builtin_amdgcn_raw_buffer_store_b128(__builtin_bit_cast(u32x4, r0), rw, fo, 0, SC1);
      __builtin_amdgcn_raw_buffer_store_b128(__builtin_bit_cast(u32x4, r1), rw, fo + 128, 0, SC1);
      vmwait<0>();
      if (lane == 0) __hip_atomic_store(flag, 1u, RLX_AGENT);
    }
    const int cons = (h & 1) == 0 ? (h >> 1) : (h < 4 ? 16 + (h >> 1) : -1);
    const int pl = lane >> 3, col = 32 * cons + 4 * (lane & 7);
    const int ocol = b * DM + col + 2 * (c & 1) + (n >> 3);
    float xx = 0.f, gg = 0.f;
#pragma unroll 1
    for (int r = 0; r < NRND; ++r) {
      const unsigned blk = RND0 + RNDS * r;
      gate(blk);
      if (ri >= 0 && lane == 0) __hip_atomic_store((gu32*)(ws + WS_RP + r * 128 + (xg * 4 + ri) * 4), ((ctl[C_LAG] + tent) & 0xffffu) | 0x10000u, RLX_AGENT);
      if (blockIdx.x == LEADER) {
        gate(blk + 1);
        const unsigned v = __hip_atomic_load((gu32*)(ws + WS_RP + r * 128 + (lane & 31) * 4), RLX_AGENT);
        const float cnt = wave_sum(v != 0u ? 1.f : 0.f), sum = wave_sum(v != 0u ? (float)(short)(v - tent) : 0.f);
        if (cnt >= 32.f && lane < 8) __hip_atomic_store((gu32*)(ws + WS_BC + (r * 8 + lane) * 128), (((int)(sum / cnt) + tent) & 0xffffu) | 0x10000u, RLX_AGENT);
      }
      if (r == RND_F && cons >= 0) {
        xx = bflo(x[ocol]);
        gu32* fp = (gu32*)(ws + WS_FP) + 9 * pl + (cons >> 1);
        SPIN(SPIN_CAP) { if (__all(__hip_atomic_load(fp, RLX_AGENT) == 1u)) break; __builtin_amdgcn_s_sleep(64); }
        asm volatile("" ::: "memory");
        gg = rbf(silu(rbf(reduce8(__builtin_bit_cast(f32x4, __builtin_amdgcn_raw_buffer_load_b128(rw, WS_PF + ((pl * NB + b) * DM + col) * 4, 0, SC1)), n & 8))));
      }
      gate(blk + 2);
      const unsigned m = __hip_atomic_load((gu32*)(ws + WS_BC + (r * 8 + xg) * 128), RLX_AGENT);
      if (m != 0u) ctl[C_SHIFT] = (int)(short)(m - tent);
    }
    if (cons < 0) { __syncthreads(); return; }
    __syncthreads();
    __builtin_amdgcn_s_sleep(POLL_DELAY);
    u32x4 pv[4];
    int po[4];
    for (int t = 0; t < 4; ++t) { pv[t] = u32x4{SENT, SENT, SENT, SENT}; po[t] = WS_P + ((b * NH + 8 * t + pl) * DM + col) * 4; }
    SPIN(16 * SPIN_CAP) {
      asm volatile("" ::: "memory");
      u32x4 v[4];
      for (int t = 0; t < 4; ++t) v[t] = __builtin_amdgcn_raw_buffer_load_b128(rw, po[t], 0, SC1);
      bool ok = true;
      for (int t = 0; t < 4; ++t) {
        const bool had = po[t] == WS_END, got = v[t][0] != SENT && v[t][1] != SENT && v[t][2] != SENT && v[t][3] != SENT;
        for (int r = 0; r < 4; ++r) pv[t][r] = had ? pv[t][r] : v[t][r];
        po[t] = got ? WS_END : po[t];
        ok = ok && got;
      }
      if (__all(ok)) break;
    }
    f32x4 s4;
    for (int r = 0; r < 4; ++r) s4[r] = (__uint_as_float(pv[0][r]) + __uint_as_float(pv[1][r])) + (__uint_as_float(pv[2][r]) + __uint_as_float(pv[3][r]));
    const float o = xx + rbf(rbf(reduce8(s4, n & 8)) * gg);
    if (lane < 32) out[ocol] = (u16)pack2(o, 0.f);
  }
}

extern "C" void kernel_launch(void* const* d_in, const int* in_sizes, int n_in, void* d_out, int out_size, void* d_ws, size_t ws_size, hipStream_t stream) {
  k_main<<<256, 512, 0, stream>>>((const u16*)d_in[0], (const u16*)d_in[1], (const u16*)d_in[2], (const u16*)d_in[3], (const u16*)d_in[4], (const u16*)d_in[6],
                                  (const u16*)d_in[7], (const u16*)d_in[8], (char*)d_ws, (u16*)d_out);
}
```

```cpp
#include <hip/hip_runtime.h>
#include <stdint.h>

typedef unsigned short u16;
typedef unsigned u32x4 __attribute__((ext_vector_type(4)));
typedef unsigned u32x2 __attribute__((ext_vector_type(2)));
typedef float f32x4 __attribute__((ext_vector_type(4)));
typedef short s16x4 __attribute__((ext_vector_type(4)));
typedef __bf16 bf16x8 __attribute__((ext_vector_type(8)));
typedef __bf16 bf16x2 __attribute__((ext_vector_type(2)));
#define LAS __attribute__((address_space(3)))
typedef __attribute__((address_space(1))) unsigned gu32;
typedef __attribute__((address_space(1))) unsigned long long gu64;
#define RLX_AGENT __ATOMIC_RELAXED, __HIP_MEMORY_SCOPE_AGENT

constexpr int NB = 8, DM = 576, NH = 32, SK = 4096, DH = 128, FF = 4608, HD = NH * DH;

constexpr int PK0 = 6, PKW = 4, PQW = 5, FFW = 7;
constexpr int XP0 = 8, XP1 = 2;
constexpr int WO_T0 = 1500, WO_DT = 110, WO_BU = 3;
constexpr int PC_T3 = 1385, PC_RK = 225, PC_RV = 221;
constexpr int PC_L0 = 20, PC_G = 16;
constexpr int G_L1 = 5, G_HS = 7, G_L2 = 8, G_GO = 10, G_D = 11;
constexpr int NRND = 5, RND0 = 11, RNDS = 4, RND_F = 2;
constexpr int POLL_DELAY = 40;
constexpr int WIN = 7;
constexpr int NPRE = 2;
constexpr int XPRE = XP0 + NPRE * XP1;

constexpr int WS_P = 0, WS_PF = WS_P + NB * NH * DM * 4, WS_GU = WS_PF + 8 * NB * DM * 4, WS_FG = WS_GU + 2 * NB * FF * 2, WS_FP = WS_FG + 1024,
              WS_HI = WS_FP + 1024, WS_HO = WS_HI + 256 * 8, WS_RP = WS_HO + 256 * 8, WS_BC = WS_RP + NRND * 128,
              WS_END = WS_BC + NRND * 8 * 128;
constexpr int LEADER = 255;
constexpr unsigned SENT = 0xffffffffu;
constexpr unsigned SPIN_CAP = 1u << 18, LDS_CAP = 1u << 22;
constexpr int ACT_LD = DM * 2 + 16;
constexpr int L_W = 0;
constexpr int L_ACT = L_W + DM * 128;
constexpr int L_SC = L_ACT + NB * ACT_LD;
constexpr int L_QP = L_SC + 4 * 16 * 64 * 2;
constexpr int L_MRG = L_QP + 3 * 128 * 4;
constexpr int L_CTL = L_MRG + 4 * 132 * 4;
constexpr int L_END = L_CTL + 32;
enum { C_Q = 0, C_BLK = 1, C_ML = 2, C_LAG = 3, C_SHIFT = 4 };

template <int I> struct IC { static constexpr int v = I; };
template <int N, int I = 0, typename Fn> __device__ __forceinline__ void sfor(Fn&& f) {
  if constexpr (I < N) { f(IC<I>{}); sfor<N, I + 1>(f); }
}
#define SCHED() __builtin_amdgcn_sched_barrier(0)
#define SPIN(cap) _Pragma("unroll 1") for (unsigned spins = 0; spins < (cap); ++spins)
template <int N> __device__ __forceinline__ void vmwait() { asm volatile("s_waitcnt vmcnt(%0)" ::"n"(N) : "memory"); }
template <int I> __device__ __forceinline__ void vmwait_l(int lvl) {
  constexpr int br = ((I & 1) << 3) | ((I & 2) << 1) | ((I & 4) >> 1) | ((I & 8) >> 3);
  asm volatile("s_cmp_gt_i32 %0, %1\n\ts_cbranch_scc1 3f\n\ts_cmp_gt_i32 %0, %2\n\ts_cbranch_scc1 2f\n\ts_cmp_gt_i32 %0, %3\n\ts_cbranch_scc1 1f\n\t"
               "s_waitcnt vmcnt(3)\n1:\n\ts_waitcnt vmcnt(4)\n2:\n\ts_waitcnt vmcnt(5)\n3:\n\ts_waitcnt vmcnt(6)"
               ::"s"(lvl), "n"(32 + br), "n"(16 + br), "n"(br) : "memory", "scc");
}
__device__ __forceinline__ unsigned now() { return (unsigned)__builtin_amdgcn_s_memrealtime(); }

__device__ __forceinline__ float bflo(unsigned x) { return __uint_as_float(x << 16); }
__device__ __forceinline__ float bfhi(unsigned x) { return __uint_as_float(x & 0xffff0000u); }
__device__ __forceinline__ unsigned pack2(float lo, float hi) { bf16x2 v = {(__bf16)lo, (__bf16)hi}; return __builtin_bit_cast(unsigned, v); }
__device__ __forceinline__ float rbf(float x) { return bflo((unsigned)__builtin_bit_cast(u16, (__bf16)x)); }
template <int CTRL> __device__ __forceinline__ float dppf(float x) {
  return __builtin_bit_cast(float, __builtin_amdgcn_mov_dpp(__builtin_bit_cast(int, x), CTRL, 0xf, 0xf, true));
}
constexpr int XOR1 = 0xB1, XOR2 = 0x4E, XOR7 = 0x141, XOR8 = 0x128, UP4 = 0x104, DOWN4 = 0x114;
template <int N> __device__ __forceinline__ unsigned bcast16(unsigned x) {
  return (unsigned)__builtin_amdgcn_mov_dpp((int)x, 0x150 + N, 0xf, 0xf, true);
}
__device__ __forceinline__ float x16(float x, bool mx) {
  auto s = __builtin_amdgcn_permlane16_swap(__float_as_uint(x), __float_as_uint(x), false, false);
  x = mx ? fmaxf(__uint_as_float(s[0]), __uint_as_float(s[1])) : __uint_as_float(s[0]) + __uint_as_float(s[1]);
  auto t = __builtin_amdgcn_permlane32_swap(__float_as_uint(x), __float_as_uint(x), false, false);
  return mx ? fmaxf(__uint_as_float(t[0]), __uint_as_float(t[1])) : __uint_as_float(t[0]) + __uint_as_float(t[1]);
}
__device__ __forceinline__ float wave_sum(float v) {
  v += dppf<XOR1>(v); v += dppf<XOR2>(v); v += dppf<XOR7>(v); v += dppf<XOR8>(v); return x16(v, false);
}
__device__ __forceinline__ float wave_max(float v) {
  v = fmaxf(v, dppf<XOR1>(v)); v = fmaxf(v, dppf<XOR2>(v)); v = fmaxf(v, dppf<XOR7>(v)); v = fmaxf(v, dppf<XOR8>(v)); return x16(v, true);
}
__device__ __forceinline__ float hi_half(float x) {
  auto t = __builtin_amdgcn_permlane32_swap(__float_as_uint(x), __float_as_uint(x), false, false);
  return __uint_as_float(t[1]);
}
template <int CH, int CL> __device__ __forceinline__ float tmerge(float lo, float hi, bool b) {
  const float u = hi + dppf<CH>(hi), v = lo + dppf<CL>(lo);
  return b ? u : v;
}
__device__ __forceinline__ float reduce8(const f32x4& v, bool b3) {
  const float u = tmerge<XOR8, XOR8>(v[0], v[1], b3), t = tmerge<XOR8, XOR8>(v[2], v[3], b3);
  auto a = __builtin_amdgcn_permlane16_swap(__float_as_uint(u), __float_as_uint(t), false, false);
  const float z = __uint_as_float(a[0]) + __uint_as_float(a[1]);
  auto e = __builtin_amdgcn_permlane32_swap(__float_as_uint(z), __float_as_uint(z), false, false);
  return __uint_as_float(e[0]) + __uint_as_float(e[1]);
}
__device__ __forceinline__ void pin(float& x) { asm volatile("" : "+v"(x)); }
__device__ __forceinline__ f32x4 mfma16(u32x4 a, u32x4 b, f32x4 c) {
  return __builtin_amdgcn_mfma_f32_16x16x32_bf16(__builtin_bit_cast(bf16x8, a), __builtin_bit_cast(bf16x8, b), c, 0, 0, 0);
}
__device__ __forceinline__ float dot2(unsigned a, unsigned b, float c) {
  return __builtin_amdgcn_fdot2_f32_bf16(__builtin_bit_cast(bf16x2, a), __builtin_bit_cast(bf16x2, b), c, false);
}
__device__ __forceinline__ float silu(float x) { return x / (1.0f + __expf(-x)); }
__device__ __forceinline__ u32x4 diag(unsigned pk, const u32x4& sel) {
  u32x4 a;
  for (int i = 0; i < 4; ++i) a[i] = __builtin_amdgcn_perm(0u, pk, sel[i]);
  return a;
}
__device__ __forceinline__ auto rsrc(const void* p, int bytes) { return __builtin_amdgcn_make_buffer_rsrc((void*)p, 0, bytes, 0x00020000); }
constexpr int NT = 2, SC1 = 16;
__device__ __forceinline__ void glds16(const void* gsrc, unsigned lds_dst) {
  unsigned keep;
  asm volatile("s_mov_b32 %0, m0\n\ts_mov_b32 m0, %2\n\ts_nop 0\n\tglobal_load_lds_dwordx4 %1, off nt\n\ts_mov_b32 m0, %0" : "=&s"(keep) : "v"(gsrc), "s"(lds_dst) : "memory");
}
__device__ __forceinline__ u32x2 trread(LAS char* p) { return __builtin_bit_cast(u32x2, __builtin_amdgcn_ds_read_tr16_b64_v4i16((LAS s16x4*)p)); }

struct LnStat { float m, den, inv; };
__device__ __forceinline__ LnStat ln_finish(float mean, float var) {
  const float den = rbf(sqrtf(rbf(rbf(var) + 1e-5f)));
  return LnStat{rbf(mean), den, 1.0f / den};
}
__device__ __forceinline__ float ln_apply(float x, const LnStat& s) {
  const float a = rbf(x - s.m), q = a * s.inv;
  return fmaf(fmaf(-q, s.den, a), s.inv, q);
}

__global__ void __launch_bounds__(512, 2) k_main(const u16* __restrict__ x, const u16* __restrict__ K, const u16* __restrict__ V, const u16* __restrict__ wq,
                                                 const u16* __restrict__ wo, const u16* __restrict__ w1, const u16* __restrict__ w3,
                                                 const u16* __restrict__ w2, char* ws, u16* __restrict__ out) {
  __shared__ __attribute__((aligned(16))) char lds_[L_END];
  LAS char* lds = (LAS char*)lds_;
  const unsigned tent = now();
  asm volatile("" ::"s"(x), "s"(K), "s"(V), "s"(wq), "s"(wo), "s"(w1), "s"(w3), "s"(w2), "s"(ws));
  const int tid = threadIdx.x, lane = tid & 63, w = __builtin_amdgcn_readfirstlane(tid >> 6);
  const int xg = blockIdx.x & 7, j0 = blockIdx.x >> 3, b = j0 >> 2;
  const int h = (j0 & 1) | (xg << 1) | ((j0 & 2) << 3), j = (j0 & 1) * 16 + (j0 >> 1);
  const char* Kb = (const char*)(K + (size_t)(b * NH + h) * SK * DH);
  const int voff = lane * 16 + w * 4096;
  u32x4 ring[16];
  auto ld = [&](const char* blk, auto ic) { constexpr int i = ic.v;
    ring[i] = __builtin_amdgcn_raw_buffer_load_b128(rsrc(blk, 65536), voff + (i & 3) * 1024, (i >> 2) * 16384, NT); };
  if (w < 4) sfor<PK0>([&](auto ic) { ld(Kb, ic); });
  SCHED();
  const int c = lane >> 4, n = lane & 15;
  volatile LAS unsigned* ctl = (volatile LAS unsigned*)(lds + L_CTL);
  if (tid < 8) ctl[tid] = 0u;
  __syncthreads();
  u32x4 sel;
  {
    const unsigned src = (n < 8) ? 0x0100u : 0x0302u; const int jj = n & 7;
    for (int i = 0; i < 4; ++i) sel[i] = ((jj >> 1) != i) ? 0x0c0c0c0cu : ((jj & 1) ? (0x0c0cu | (src << 16)) : (0x0c0c0000u | src));
  }
  if (w < 4) {
    const char* Vb = (const char*)(V + (size_t)(b * NH + h) * SK * DH);
    u32x4 pre[16 * NPRE];
    sfor<16 - PK0>([&](auto ic) { SCHED(); vmwait<PKW - 1>(); SCHED(); ld(Kb, IC<ic.v + PK0>{}); });
    sfor<16 * NPRE>([&](auto ic) { constexpr int i = ic.v; SCHED(); vmwait<PKW - 1>(); SCHED();
      pre[i] = __builtin_amdgcn_raw_buffer_load_b128(rsrc(Kb + 65536, NPRE * 65536), voff + (i & 3) * 1024, (i >> 2) * 16384, NT); });
    SCHED();
    __builtin_amdgcn_s_setprio(3);
    SPIN(LDS_CAP) { if (ctl[C_Q] >= 3u) break; __builtin_amdgcn_s_sleep(1); }
    asm volatile("" ::: "memory");
    unsigned qd[4];
    {
      f32x4 a = {0.f, 0.f, 0.f, 0.f}, e = a;
      for (int i = 0; i < 3; ++i) { a += *(LAS f32x4*)(lds + L_QP + i * 512 + 32 * n); e += *(LAS f32x4*)(lds + L_QP + i * 512 + 32 * n + 16); }
      qd[0] = pack2(a[0], a[1]); qd[1] = pack2(a[2], a[3]); qd[2] = pack2(e[0], e[1]); qd[3] = pack2(e[2], e[3]);
    }
    LAS u16* sc = (LAS u16*)(lds + L_SC) + w * 1024 + lane;
    float mx = -INFINITY;
    int lvl = PC_L0;
    unsigned due = tent + (PC_T3 - PC_RK);
    auto pace = [&](int ticks) {
      due += ticks;
      const int shift = __builtin_amdgcn_readfirstlane(ctl[C_SHIFT]);
      const int lag = min(max((int)(now() - due), -4096), 4096);
      if (w == 0) ctl[C_LAG] = lag;
      lvl = min(max(PC_L0 + ((lag - shift) * PC_G >> 6), 0), 48);
    };
    const bool b0 = n & 1, b1 = n & 2, b2 = n & 4, b3 = n & 8;
    auto kblock = [&](const char* cur, const char* nxt, int blk, auto mode) {
      float t0, t1, t2, t3;
      sfor<16>([&](auto ic) { constexpr int i = ic.v;
        const u32x4 kv = mode.v >= 2 ? pre[(mode.v >= 2 ? mode.v - 2 : 0) * 16 + i] : ring[i];
        if constexpr (mode.v == 0 || (mode.v == -1 && i + WIN >= XPRE)) {
          SCHED(); vmwait_l<i>(lvl); SCHED(); if constexpr (i + WIN >= 16) ld(nxt, IC<i + WIN - 16>{}); else ld(cur, IC<i + WIN>{}); SCHED(); }
        float d = dot2(kv[0], qd[0], 0.f); d = dot2(kv[1], qd[1], d); d = dot2(kv[2], qd[2], d); d = dot2(kv[3], qd[3], d);
        pin(d);
        SCHED();
        if constexpr (mode.v == 1 && i < XP0) { ld(nxt, ic); SCHED(); }
        if constexpr (mode.v >= 2 && i % (16 / XP1) == 0) { ld(nxt, IC<XP0 + (mode.v - 2) * XP1 + i / (16 / XP1)>{}); SCHED(); }
        if constexpr (!(i & 1)) t0 = d;
        else {
          d = tmerge<XOR1, XOR1>(t0, d, b0);
          if constexpr (!(i & 2)) t1 = d;
          else {
            d = tmerge<XOR2, XOR2>(t1, d, b1);
            if constexpr (!(i & 4)) t2 = d;
            else {
              d = tmerge<DOWN4, UP4>(t2, d, b2);
              if constexpr (!(i & 8)) t3 = d;
              else {
                d = tmerge<XOR8, XOR8>(t3, d, b3);
                d = rbf(rbf(d) * 0.08838834764831845f);
                mx = fmaxf(mx, d);
                sc[blk * 64] = (u16)(__float_as_uint(d) >> 16);
                if (w == 0) ctl[C_BLK] = blk + 1;
              }
            }
          }
        }
      });
    };
    constexpr int B0 = NPRE + 1;
    kblock(Kb, Kb + B0 * 65536, 0, IC<1>{});
    sfor<NPRE>([&](auto pc) { kblock(Kb, Kb + B0 * 65536, 1 + pc.v, IC<2 + pc.v>{}); });
    kblock(Kb + B0 * 65536, Kb + (B0 + 1) * 65536, B0, IC<-1>{});
    pace(PC_RK);
#pragma unroll 1
    for (int blk = B0 + 1; blk < 16; ++blk) { kblock(Kb + blk * 65536, blk < 15 ? Kb + (blk + 1) * 65536 : Vb, blk, IC<0>{}); pace(PC_RK); }
    mx = wave_max(mx);
    float l = 0.f;
    f32x4 acc = {0.f, 0.f, 0.f, 0.f};
    float s = bflo(sc[0]);
    LAS float* mr = (LAS float*)(lds + L_MRG) + w * 132;
    auto vblock = [&](const char* cur, const char* nxt, int blk, auto last) {
      if (w == 0) ctl[C_BLK] = 16 + blk;
      const float p = __expf(s - mx);
      l += p;
      const float ph = rbf(p);
      const unsigned pk = pack2(ph, p - ph);
      if constexpr (!last.v) s = bflo(sc[(blk + 1) * 64]);
      else {
        l = wave_sum(l);
        if (lane == 0) { mr[0] = mx; mr[1] = l; }
        asm volatile("s_waitcnt lgkmcnt(0)" ::: "memory");
        if (lane == 0) __hip_atomic_fetch_add((LAS unsigned*)(lds + L_CTL) + C_ML, 1u, __ATOMIC_RELAXED, __HIP_MEMORY_SCOPE_WORKGROUP);
      }
      sfor<16>([&](auto ic) { constexpr int i = ic.v;
        const u32x4 a = diag(bcast16<i>(pk), sel), vv = ring[i];
        SCHED();
        if constexpr (!last.v || i + WIN < 16) vmwait_l<i>(lvl);
        SCHED();
        if constexpr (i + WIN < 16) ld(cur, IC<i + WIN>{}); else if constexpr (!last.v) ld(nxt, IC<i + WIN - 16>{});
        SCHED();
        acc = mfma16(a, vv, acc);
        SCHED();
      });
    };
#pragma unroll 1
    for (int blk = 0; blk < 15; ++blk) { vblock(Vb + blk * 65536, Vb + (blk + 1) * 65536, blk, IC<0>{}); pace(PC_RV); }
    vblock(Vb + 15 * 65536, Vb, 15, IC<1>{});
    for (int r = 0; r < 2; ++r) {
      auto t = __builtin_amdgcn_permlane32_swap(__float_as_uint(acc[r]), __float_as_uint(acc[r + 2]), false, false);
      mr[4 + (4 * (c & 1) + 2 * (c >> 1) + r) * 16 + n] = __uint_as_float(t[0]) + __uint_as_float(t[1]);
    }
    __syncthreads();
  } else if (w < 7) {
    const int i4 = w - 4;
    {
      const unsigned* xr = (const unsigned*)(x + b * DM);
      unsigned d[5];
      for (int t = 0; t < 4; ++t) d[t] = xr[lane + 64 * t];
      d[4] = xr[256 + (lane & 31)];
      const u16* xe = x + b * DM + 192 * i4 + c;
      const unsigned e0 = xe[8 * n], e1 = xe[8 * n + 4], e2 = xe[128 + 8 * (n & 7)], e3 = xe[128 + 8 * (n & 7) + 4];
      SCHED();
      u32x4 wr[48];
      const auto rq = rsrc(wq, DM * HD * 2);
      const int vo = (4 * (n >> 3) + c) * (HD * 2) + (n & 7) * 16;
      const int so = 192 * i4 * (HD * 2) + h * 256;
      auto ldq = [&](auto ic) { constexpr int i = ic.v;
        if constexpr (i < 48) wr[i] = __builtin_amdgcn_raw_buffer_load_b128(rq, vo + (i & 1) * 128, so + (i >> 1) * 8 * (HD * 2), NT); };
      sfor<PQW>(ldq);
      SCHED();
      float sm = 0.f, sq = 0.f;
      for (int t = 0; t < 5; ++t) sm += (t < 4 || lane < 32) ? bflo(d[t]) + bfhi(d[t]) : 0.f;
      const float mean = wave_sum(sm) * (1.0f / DM);
      for (int t = 0; t < 5; ++t) { const float a = bflo(d[t]) - mean, e = bfhi(d[t]) - mean; sq += (t < 4 || lane < 32) ? a * a + e * e : 0.f; }
      const LnStat st = ln_finish(mean, wave_sum(sq) * (1.0f / DM));
      const unsigned pk0 = pack2(ln_apply(bflo(e0), st), ln_apply(bflo(e1), st)), pk1 = pack2(ln_apply(bflo(e2), st), ln_apply(bflo(e3), st));
      f32x4 a0 = {0.f, 0.f, 0.f, 0.f}, a1 = a0;
      sfor<24>([&](auto sc_) { constexpr int s = sc_.v;
        const u32x4 a = diag(bcast16<s & 15>(s < 16 ? pk0 : pk1), sel);
        a0 = mfma16(a, wr[2 * s], a0); SCHED(); ldq(IC<2 * s + PQW>{}); SCHED();
        a1 = mfma16(a, wr[2 * s + 1], a1); SCHED(); ldq(IC<2 * s + 1 + PQW>{}); SCHED(); });
      f32x4 o0, o1;
      for (int r = 0; r < 4; ++r) { o0[r] = a0[r] + hi_half(dppf<XOR8>(a0[r])); o1[r] = a1[r] + hi_half(dppf<XOR8>(a1[r])); }
      if (c < 2 && n < 8) { *(LAS f32x4*)(lds + L_QP + i4 * 512 + 32 * n + 16 * c) = o0; *(LAS f32x4*)(lds + L_QP + i4 * 512 + 256 + 32 * n + 16 * c) = o1; }
      asm volatile("s_waitcnt lgkmcnt(0)" ::: "memory");
      if (lane == 0) __hip_atomic_fetch_add((LAS unsigned*)(lds + L_CTL) + C_Q, 1u, __ATOMIC_RELAXED, __HIP_MEMORY_SCOPE_WORKGROUP);
    }
    u32x4 wr[48];
    const auto ro = rsrc(wo, HD * DM * 2);
    const int vo = (4 * (n >> 3) + c) * (DM * 2) + (n & 7) * 16;
    const int so = 128 * h * (DM * 2) + 384 * i4;
    sfor<48>([&](auto ic) { constexpr int i = ic.v;
      if constexpr (i % WO_BU == 0) SPIN(LDS_CAP) { if (now() - tent >= (unsigned)(WO_T0 + i * WO_DT)) break; __builtin_amdgcn_s_sleep(2); }
      wr[i] = __builtin_amdgcn_raw_buffer_load_b128(ro, vo + (i % 3) * 128, so + (i / 3) * 8 * (DM * 2), NT);
      SCHED(); if constexpr (i % WO_BU == WO_BU - 1) vmwait<WO_BU>(); SCHED(); });
    const LAS float* mr = (const LAS float*)(lds + L_MRG);
    SPIN(LDS_CAP) { if (ctl[C_ML] >= 4u) break; __builtin_amdgcn_s_sleep(4); }
    asm volatile("" ::: "memory");
    float f4[4], M = -INFINITY, L = 0.f;
    for (int i = 0; i < 4; ++i) { f4[i] = mr[i * 132]; M = fmaxf(M, f4[i]); }
    for (int i = 0; i < 4; ++i) { f4[i] = __expf(f4[i] - M); L += f4[i] * mr[i * 132 + 1]; }
    float linv = 1.0f / L;
    for (int i = 0; i < 4; ++i) pin(f4[i]);
    pin(linv);
    __syncthreads();
    float oa = 0.f, ob = 0.f;
    for (int i = 0; i < 4; ++i) { oa += f4[i] * mr[i * 132 + 4 + c * 16 + n]; ob += f4[i] * mr[i * 132 + 4 + (4 + c) * 16 + n]; }
    const unsigned pk = pack2(oa * linv, ob * linv);
    f32x4 a3[3] = {{0.f, 0.f, 0.f, 0.f}, {0.f, 0.f, 0.f, 0.f}, {0.f, 0.f, 0.f, 0.f}};
    sfor<16>([&](auto sc_) { constexpr int s = sc_.v;
      const u32x4 a = diag(bcast16<s>(pk), sel);
      for (int t = 0; t < 3; ++t) a3[t] = mfma16(a, wr[3 * s + t], a3[t]); });
    const int po = WS_P + ((b * NH + h) * DM + 192 * i4 + 8 * n + 4 * (c & 1)) * 4;
    u32x4 o01, o2;
    for (int r = 0; r < 4; ++r) {
      auto t = __builtin_amdgcn_permlane32_swap(__float_as_uint(a3[0][r]), __float_as_uint(a3[1][r]), false, false);
      const unsigned v = __float_as_uint(__uint_as_float(t[0]) + dppf<XOR8>(__uint_as_float(t[1])));
      o01[r] = v == SENT ? SENT - 1u : v;
    }
    if (n < 8) __builtin_amdgcn_raw_buffer_store_b128(o01, rsrc(ws, WS_END), po + 256 * (c >> 1), 0, SC1);
    for (int r = 0; r < 4; ++r) {
      const unsigned v = __float_as_uint(a3[2][r] + hi_half(dppf<XOR8>(a3[2][r])));
      o2[r] = v == SENT ? SENT - 1u : v;
    }
    if (n < 8 && c < 2) __builtin_amdgcn_raw_buffer_store_b128(o2, rsrc(ws, WS_END), po + 512, 0, SC1);
  } else {
    const auto rw = rsrc(ws, WS_END);
    auto gate = [&](unsigned blocks) { SPIN(LDS_CAP) { if (ctl[C_BLK] >= blocks) break; __builtin_amdgcn_s_sleep(8); } };
    const bool gu = j < 18, dn = j >= 18 && j < 27;
    const int i0 = (14 - xg) & 7, iu = j >= 16 ? i0 : (j >> 1) + ((j >> 1) >= i0);
    const int mat = j & 1, u = 9 * xg + iu, hb = j - 18;
    gu32* flag = (gu32*)(ws + (gu ? WS_FG + (2 * u + mat) * 4 : WS_FP + (xg * 9 + hb) * 4));
    if (j < 27) {
      const int ldw = gu ? FF * 2 : DM * 2;
      const char* src = (gu ? (const char*)((mat ? w3 : w1) + 64 * u) : (const char*)(w2 + (size_t)576 * xg * DM + 64 * hb)) + (lane >> 3) * ldw + (lane & 7) * 16;
#pragma unroll 1
      for (int i = 0; i < 72; ++i) { glds16(src, (unsigned)(size_t)(lds + L_W) + i * 1024); src += 8 * ldw; vmwait<FFW - 1>(); }
    }
    auto echo_of = [](unsigned long long y) { return (unsigned)y & 0xffffffu; };
    auto chal_of = [](unsigned long long y) { return (unsigned)(y >> 24) & 0xffffffu; };
    auto word = [](unsigned echo, unsigned ch, unsigned hint) {
      return (unsigned long long)(echo & 0xffffffu) | ((unsigned long long)ch << 24) | ((unsigned long long)(hint & 0xffffu) << 48); };
    const int ri = j0 == 2 ? 0 : j0 == 12 ? 1 : j0 == 23 ? 2 : j0 == 29 ? 3 : -1;
    gu64* hi = (gu64*)(ws + WS_HI); gu64* ho = (gu64*)(ws + WS_HO);
    const unsigned long long yo = __hip_atomic_load(ho + blockIdx.x, RLX_AGENT), yi = __hip_atomic_load(hi + blockIdx.x, RLX_AGENT);
    {
      const u32x4 e = {SENT, SENT, SENT, SENT};
      for (int t = 0; t < 3; ++t)
        if (t < 2 || lane < 16) __builtin_amdgcn_raw_buffer_store_b128(e, rw, WS_P + (b * NH + h) * DM * 4 + (t * 64 + lane) * 16, 0, SC1);
      if (lane == 0 && j < 27) __hip_atomic_store(flag, 0u, RLX_AGENT);
      if (ri >= 0 && lane < NRND) __hip_atomic_store((gu32*)(ws + WS_RP + lane * 128 + (xg * 4 + ri) * 4), 0u, RLX_AGENT);
      if (blockIdx.x == LEADER && lane < NRND * 8) __hip_atomic_store((gu32*)(ws + WS_BC + lane * 128), 0u, RLX_AGENT);
    }
    const unsigned f0 = echo_of(yo), old = chal_of(yi);
    const unsigned chal = (f0 + (((old - f0 - 1u) & 0xffffffu) < 2u ? 3u : 1u)) & 0xffffffu;
    auto tile = [&](f32x4& r0, f32x4& r1) {
      const int q = n >> 2, p = n & 3;
      LAS char* wp = lds + L_W + (8 * c + q) * 128 + (gu ? 16 * p : 8 * p);
      const int o1 = gu ? 8 : 32, o2 = 64;
      const LAS char* ap = lds + L_ACT + (n & 7) * ACT_LD + 16 * c;
      f32x4 d[4];
      for (int T = 0; T < 4; ++T) d[T] = f32x4{0.f, 0.f, 0.f, 0.f};
#pragma unroll 2
      for (int s = 0; s < 18; ++s) {
        const u32x4 av = *(const LAS u32x4*)(ap + 64 * s);
        for (int T = 0; T < 4; ++T) {
          LAS char* a = wp + s * 4096 + (T & 1) * o1 + (T >> 1) * o2;
          const u32x2 lo = trread(a), hi2 = trread(a + 512);
          d[T] = mfma16(u32x4{lo[0], lo[1], hi2[0], hi2[1]}, av, d[T]);
        }
      }
      if (gu) { r0 = n < 8 ? d[0] : d[2]; r1 = n < 8 ? d[1] : d[3]; }
      else { r0 = n < 8 ? d[0] : d[1]; r1 = n < 8 ? d[2] : d[3]; }
    };
    u32x4 xd[9];
    if (gu) for (int t = 0; t < 9; ++t) xd[t] = *(const u32x4*)(x + (lane >> 3) * DM + 8 * ((lane & 7) + 8 * t));
    vmwait<0>();
    if (blockIdx.x != LEADER && lane == 0) __hip_atomic_store(hi + blockIdx.x, word(chal_of(yo), chal, tent), RLX_AGENT);
    if (gu) {
      float sm = 0.f, sq = 0.f;
      for (int t = 0; t < 9; ++t) for (int e = 0; e < 4; ++e) sm += bflo(xd[t][e]) + bfhi(xd[t][e]);
      sm += dppf<XOR1>(sm); sm += dppf<XOR2>(sm); sm += dppf<XOR7>(sm);
      const float mean = sm * (1.0f / DM);
      for (int t = 0; t < 9; ++t) for (int e = 0; e < 4; ++e) { const float a = bflo(xd[t][e]) - mean, d = bfhi(xd[t][e]) - mean; sq += a * a + d * d; }
      sq += dppf<XOR1>(sq); sq += dppf<XOR2>(sq); sq += dppf<XOR7>(sq);
      const LnStat st = ln_finish(mean, sq * (1.0f / DM));
      for (int t = 0; t < 9; ++t) {
        u32x4 v;
        for (int e = 0; e < 4; ++e) v[e] = pack2(ln_apply(bflo(xd[t][e]), st), ln_apply(bfhi(xd[t][e]), st));
        *(LAS u32x4*)(lds + L_ACT + (lane >> 3) * ACT_LD + 16 * ((lane & 7) + 8 * t)) = v;
      }
      f32x4 r0, r1;
      tile(r0, r1);
      const u32x4 o = {pack2(r0[0], r0[1]), pack2(r0[2], r0[3]), pack2(r1[0], r1[1]), pack2(r1[2], r1[3])};
      __builtin_amdgcn_raw_buffer_store_b128(o, rw, WS_GU + ((mat * NB + (n & 7)) * FF + 64 * u + 32 * (n >> 3) + 8 * c) * 2, 0, SC1);
      vmwait<0>();
      if (lane == 0) __hip_atomic_store(flag, 1u, RLX_AGENT);
    }
    if (blockIdx.x == LEADER) {
      unsigned long long y[4]; unsigned cl[4], sent[4];
      gate(G_L1);
      for (int q = 0; q < 4; ++q) { y[q] = __hip_atomic_load(hi + q * 64 + lane, RLX_AGENT); cl[q] = (echo_of(y[q]) + 1u) & 0xffffffu; sent[q] = ~0u; }
      SPIN(SPIN_CAP) {
        bool ok = true;
        for (int q = 0; q < 4; ++q) ok = ok && (echo_of(y[q]) == cl[q] || q * 64 + lane == LEADER);
        if (__all(ok)) break;
        for (int q = 0; q < 4; ++q)
          if (sent[q] != chal_of(y[q])) { sent[q] = chal_of(y[q]); __hip_atomic_store(ho + q * 64 + lane, word(sent[q], cl[q], 0u), RLX_AGENT); }
        gate(spins == 0 ? G_L1 + 1 : G_L2);
        __builtin_amdgcn_s_sleep(32);
        for (int q = 0; q < 4; ++q) y[q] = __hip_atomic_load(hi + q * 64 + lane, RLX_AGENT);
      }
      float ls = 0.f;
      for (int q = 0; q < 4; ++q) ls += q * 64 + lane == LEADER ? 0.f : (float)(short)((unsigned)(y[q] >> 48) - tent);
      const int mean = (int)(wave_sum(ls) * (1.0f / 255.f));
      for (int q = 0; q < 4; ++q) __hip_atomic_store(ho + q * 64 + lane, word(chal_of(y[q]) + 1u, cl[q], mean + tent), RLX_AGENT);
      ctl[C_SHIFT] = mean;
    } else {
      gate(G_HS);
      SPIN(SPIN_CAP) {
        const unsigned long long y = __hip_atomic_load(ho + blockIdx.x, RLX_AGENT);
        if (((__builtin_amdgcn_readfirstlane(echo_of(y)) - chal) & 0xffffffu) < 2u) {
          if (lane == 0) __hip_atomic_store(hi + blockIdx.x, word(chal_of(y), chal, ctl[C_LAG] + tent), RLX_AGENT);
          break;
        }
        __builtin_amdgcn_s_sleep(64);
      }
      gate(G_GO);
      SPIN(SPIN_CAP) {
        const unsigned long long y = __hip_atomic_load(ho + blockIdx.x, RLX_AGENT);
        if (__builtin_amdgcn_readfirstlane(echo_of(y)) == ((chal + 1u) & 0xffffffu)) { ctl[C_SHIFT] = (int)(short)((unsigned)(y >> 48) - tent); break; }
        __builtin_amdgcn_s_sleep(64);
      }
    }
    asm volatile("" ::: "memory");
    if (dn) {
      gate(G_D);
      gu32* fg = (gu32*)(ws + WS_FG) + 18 * xg + (lane < 18 ? lane : 0);
      SPIN(SPIN_CAP) { if (__all(__hip_atomic_load(fg, RLX_AGENT) == 1u)) break; __builtin_amdgcn_s_sleep(64); }
      asm volatile("" ::: "memory");
#pragma unroll 1
      for (int t3 = 0; t3 < 3; ++t3) {
        u32x4 gv[3], uv[3]; int lo[3];
        for (int t = 0; t < 3; ++t) {
          const int q = lane + 64 * (3 * t3 + t), bb = q / 72, ch = q - 72 * bb, go = WS_GU + (bb * FF + 576 * xg + 8 * ch) * 2;
          gv[t] = __builtin_amdgcn_raw_buffer_load_b128(rw, go, 0, SC1); uv[t] = __builtin_amdgcn_raw_buffer_load_b128(rw, go + NB * FF * 2, 0, SC1);
          lo[t] = L_ACT + bb * ACT_LD + 16 * ch;
        }
        for (int t = 0; t < 3; ++t) {
          u32x4 hv;
          for (int e = 0; e < 4; ++e) hv[e] = pack2(rbf(silu(bflo(gv[t][e]))) * bflo(uv[t][e]), rbf(silu(bfhi(gv[t][e]))) * bfhi(uv[t][e]));
          *(LAS u32x4*)(lds + lo[t]) = hv;
        }
      }
      f32x4 r0, r1;
      tile(r0, r1);
      const int fo = WS_PF + ((xg * NB + (n & 7)) * DM + 64 * hb + 16 * (n >> 3) + 4 * c) * 4;
      __builtin_amdgcn_raw_buffer_store_b128(__builtin_bit_cast(u32x4, r0), rw, fo, 0, SC1);
      __builtin_amdgcn_raw_buffer_store_b128(__builtin_bit_cast(u32x4, r1), rw, fo + 128, 0, SC1);
      vmwait<0>();
      if (lane == 0) __hip_atomic_store(flag, 1u, RLX_AGENT);
    }
    const int cons = (h & 1) == 0 ? (h >> 1) : (h < 4 ? 16 + (h >> 1) : -1);
    const int pl = lane >> 3, col = 32 * cons + 4 * (lane & 7);
    const int ocol = b * DM + col + 2 * (c & 1) + (n >> 3);
    float xx = 0.f, gg = 0.f;
#pragma unroll 1
    for (int r = 0; r < NRND; ++r) {
      const unsigned blk = RND0 + RNDS * r;
      gate(blk);
      if (ri >= 0 && lane == 0) __hip_atomic_store((gu32*)(ws + WS_RP + r * 128 + (xg * 4 + ri) * 4), ((ctl[C_LAG] + tent) & 0xffffu) | 0x10000u, RLX_AGENT);
      if (blockIdx.x == LEADER) {
        gate(blk + 1);
        const unsigned v = __hip_atomic_load((gu32*)(ws + WS_RP + r * 128 + (lane & 31) * 4), RLX_AGENT);
        const float cnt = wave_sum(v != 0u ? 1.f : 0.f), sum = wave_sum(v != 0u ? (float)(short)(v - tent) : 0.f);
        if (cnt >= 32.f && lane < 8) __hip_atomic_store((gu32*)(ws + WS_BC + (r * 8 + lane) * 128), (((int)(sum / cnt) + tent) & 0xffffu) | 0x10000u, RLX_AGENT);
      }
      if (r == RND_F && cons >= 0) {
        xx = bflo(x[ocol]);
        gu32* fp = (gu32*)(ws + WS_FP) + 9 * pl + (cons >> 1);
        SPIN(SPIN_CAP) { if (__all(__hip_atomic_load(fp, RLX_AGENT) == 1u)) break; __builtin_amdgcn_s_sleep(64); }
        asm volatile("" ::: "memory");
        gg = rbf(silu(rbf(reduce8(__builtin_bit_cast(f32x4, __builtin_amdgcn_raw_buffer_load_b128(rw, WS_PF + ((pl * NB + b) * DM + col) * 4, 0, SC1)), n & 8))));
      }
      gate(blk + 2);
      const unsigned m = __hip_atomic_load((gu32*)(ws + WS_BC + (r * 8 + xg) * 128), RLX_AGENT);
      if (m != 0u) ctl[C_SHIFT] = (int)(short)(m - tent);
    }
    if (cons < 0) { __syncthreads(); return; }
    __syncthreads();
    __builtin_amdgcn_s_sleep(POLL_DELAY);
    u32x4 pv[4];
    int po[4];
    for (int t = 0; t < 4; ++t) { pv[t] = u32x4{SENT, SENT, SENT, SENT}; po[t] = WS_P + ((b * NH + 8 * t + pl) * DM + col) * 4; }
    SPIN(16 * SPIN_CAP) {
      asm volatile("" ::: "memory");
      u32x4 v[4];
      for (int t = 0; t < 4; ++t) v[t] = __builtin_amdgcn_raw_buffer_load_b128(rw, po[t], 0, SC1);
      bool ok = true;
      for (int t = 0; t < 4; ++t) {
        const bool had = po[t] == WS_END, got = v[t][0] != SENT && v[t][1] != SENT && v[t][2] != SENT && v[t][3] != SENT;
        for (int r = 0; r < 4; ++r) pv[t][r] = had ? pv[t][r] : v[t][r];
        po[t] = got ? WS_END : po[t];
        ok = ok && got;
      }
      if (__all(ok)) break;
    }
    f32x4 s4;
    for (int r = 0; r < 4; ++r) s4[r] = (__uint_as_float(pv[0][r]) + __uint_as_float(pv[1][r])) + (__uint_as_float(pv[2][r]) + __uint_as_float(pv[3][r]));
    const float o = xx + rbf(rbf(reduce8(s4, n & 8)) * gg);
    if (lane < 32) out[ocol] = (u16)pack2(o, 0.f);
  }
}

extern "C" void kernel_launch(void* const* d_in, const int* in_sizes, int n_in, void* d_out, int out_size, void* d_ws, size_t ws_size, hipStream_t stream) {
  k_main<<<256, 512, 0, stream>>>((const u16*)d_in[0], (const u16*)d_in[1], (const u16*)d_in[2], (const u16*)d_in[3], (const u16*)d_in[4], (const u16*)d_in[6],
                                  (const u16*)d_in[7], (const u16*)d_in[8], (char*)d_ws, (u16*)d_out);
}
```
